```python
import torch

def _softmax(x, axis=-1):
    return torch.softmax(x, dim=axis)
def _gelu(x):
    return torch.nn.functional.gelu(x, approximate="tanh")
def _silu(x):
    return torch.nn.functional.silu(x)
def _sigmoid(x):
    return torch.sigmoid(x)
def _layernorm(x, axis=-1, eps=1e-5):
    m = x.mean(dim=axis, keepdim=True)
    v = x.var(dim=axis, keepdim=True, unbiased=False)
    return (x - m) / torch.sqrt(v + eps)
def _scatter_add(tgt, idx, src, axis=0):
    return tgt.clone().index_add_(axis, idx, src)
def _segment_sum(src, seg, n):
    out = torch.zeros((n,) + src.shape[1:], dtype=src.dtype)
    return out.index_add_(0, seg, src)
def _pad(x, pw):
    flat = tuple(v for pair in reversed(pw) for v in pair)
    return torch.nn.functional.pad(x, flat)

kernel_name = 'recurrent_scan__softmax_full_mha__dense_activation_squared_relu__prompt'

def setup_inputs():

    _w = (lambda a: (lambda t: t.to(torch.bfloat16) if t.is_floating_point() else t)(torch.as_tensor(a)))
    def _A_setup():
        B, S, D = 1, 2048, 640
        return {
            "x": _w((lambda *s: torch.randn(*s, dtype=torch.bfloat16) * 0.05)(B, S, D)),
            "wqkv": _w((lambda *s: torch.randn(*s, dtype=torch.bfloat16) * 0.05)(D, 3 * D)),
            "wo": _w((lambda *s: torch.randn(*s, dtype=torch.bfloat16) * 0.05)(D, D)),
        }
    _A_in = _A_setup()

    def _B_setup():
        B, S, D, F = 1, 2048, 640, 1536
        return {
            "x": _w((lambda *s: torch.randn(*s, dtype=torch.bfloat16) * 0.05)(B, S, D)),
            "w1": _w((lambda *s: torch.randn(*s, dtype=torch.bfloat16) * 0.05)(D, F)),
            "b1": _w((lambda *s: torch.randn(*s, dtype=torch.bfloat16) * 0.8)(F)),
            "w2": _w((lambda *s: torch.randn(*s, dtype=torch.bfloat16) * 0.05)(F, D)),
            "b2": _w((lambda *s: torch.randn(*s, dtype=torch.bfloat16) * 3.2)(D)),
        }
    _B_in = _B_setup()

    return {
        "A_x": _A_in["x"],
        "A_wqkv": _A_in["wqkv"],
        "A_wo": _A_in["wo"],
        "B_x": _B_in["x"],
        "B_w1": _B_in["w1"],
        "B_b1": _B_in["b1"],
        "B_w2": _B_in["w2"],
        "B_b2": _B_in["b2"],
    }

def reference(A_x, A_wqkv, A_wo, B_x, B_w1, B_b1, B_w2, B_b2):
    def _A_ref(x, wqkv, wo):
        B, S, D = x.shape
        H = 32; Dh = D // H
        qkv = (lambda x, *d: x.permute(*d))((x @ wqkv).reshape(B, S, 3, H, Dh), 2, 0, 3, 1, 4)
        q, k, v = qkv[0], qkv[1], qkv[2]
        scale = Dh ** -0.5
        scores = torch.einsum("bhqd,bhkd->bhqk", q, k) * scale
        p = _softmax(scores, axis=-1)
        o = torch.einsum("bhqk,bhkd->bhqd", p, v)
        return (lambda x, *d: x.permute(*d))(o, 0, 2, 1, 3).reshape(B, S, D) @ wo

    def _B_ref(x, w1, b1, w2, b2):
        h = torch.einsum("bsd,df->bsf", x, w1) + b1
        zero = torch.zeros_like(h)
        r = torch.where(h > zero, h, zero)
        h = r * r
        out = torch.einsum("bsf,fd->bsd", h, w2) + b2
        return out

    x = A_x
    xn = _layernorm(x, axis=-1)
    s = _A_ref(xn, A_wqkv, A_wo)
    g = _silu(_B_ref(xn, B_w1, B_b1, B_w2, B_b2))
    return x + s * g
```

```cpp
#include <hip/hip_runtime.h>
#include <hip/hip_bf16.h>

typedef unsigned short u16;
static constexpr int S = 2048, D = 640, H = 32, DH = 20, F = 1536;

__device__ __forceinline__ float bf2f(u16 v) { return __uint_as_float(((unsigned)v) << 16); }

__global__ void k_ln(const u16* x, float* xn) {
  int row = blockIdx.x;
  __shared__ float red[2][64];
  float s = 0.f;
  for (int i = threadIdx.x; i < D; i += 64) s += bf2f(x[row * D + i]);
  for (int o = 32; o; o >>= 1) s += __shfl_xor(s, o);
  float m = s / D, v = 0.f;
  for (int i = threadIdx.x; i < D; i += 64) { float d = bf2f(x[row * D + i]) - m; v += d * d; }
  for (int o = 32; o; o >>= 1) v += __shfl_xor(v, o);
  float r = rsqrtf(v / D + 1e-5f);
  for (int i = threadIdx.x; i < D; i += 64) xn[row * D + i] = (bf2f(x[row * D + i]) - m) * r;
}

__global__ void k_gemm(const float* a, const u16* w, float* c, int K, int N, const u16* bias, int mode) {
  __shared__ float as[16][17], bs[16][17];
  int tx = threadIdx.x & 15, ty = threadIdx.x >> 4;
  int row = blockIdx.y * 16 + ty, col = blockIdx.x * 16 + tx;
  float acc = 0.f;
  for (int k0 = 0; k0 < K; k0 += 16) {
    as[ty][tx] = a[row * K + k0 + tx];
    bs[ty][tx] = bf2f(w[(k0 + ty) * N + col]);
    __syncthreads();
    for (int k = 0; k < 16; ++k) acc += as[ty][k] * bs[k][tx];
    __syncthreads();
  }
  if (mode >= 1) acc += bf2f(bias[col]);
  if (mode == 1) { acc = fmaxf(acc, 0.f); acc *= acc; }
  c[row * N + col] = acc;
}

__global__ void k_attn(const float* qkv, float* o) {
  int h = blockIdx.y, q = blockIdx.x * 64 + threadIdx.x;
  float qv[DH], acc[DH];
  for (int d = 0; d < DH; ++d) { qv[d] = qkv[q * 3 * D + h * DH + d] * 0.22360679775f; acc[d] = 0.f; }
  float m = -INFINITY, l = 0.f;
  for (int k = 0; k < S; ++k) {
    const float* kp = qkv + k * 3 * D + D + h * DH;
    float s = 0.f;
    for (int d = 0; d < DH; ++d) s += qv[d] * kp[d];
    float mn = fmaxf(m, s), al = __expf(m - mn), p = __expf(s - mn);
    l = l * al + p;
    for (int d = 0; d < DH; ++d) acc[d] = acc[d] * al + p * kp[D + d];
    m = mn;
  }
  for (int d = 0; d < DH; ++d) o[q * D + h * DH + d] = acc[d] / l;
}

__global__ void k_final(const u16* x, const float* s, const float* g, __hip_bfloat16* out) {
  int i = blockIdx.x * 256 + threadIdx.x;
  float gv = g[i];
  out[i] = __float2bfloat16(bf2f(x[i]) + s[i] * gv / (1.f + __expf(-gv)));
}

extern "C" void kernel_launch(void* const* d_in, const int* in_sizes, int n_in, void* d_out, int out_size,
                              void* d_ws, size_t ws_size, hipStream_t stream) {
  const u16 *x = (const u16*)d_in[0], *wqkv = (const u16*)d_in[1], *wo = (const u16*)d_in[2];
  const u16 *w1 = (const u16*)d_in[4], *b1 = (const u16*)d_in[5], *w2 = (const u16*)d_in[6], *b2 = (const u16*)d_in[7];
  float* ws = (float*)d_ws;
  float *xn = ws, *qkv = xn + S * D, *hh = qkv + S * 3 * D, *o = hh + S * F, *s = o + S * D, *g = s + S * D;
  k_ln<<<S, 64, 0, stream>>>(x, xn);
  k_gemm<<<dim3(3 * D / 16, S / 16), 256, 0, stream>>>(xn, wqkv, qkv, D, 3 * D, nullptr, 0);
  k_gemm<<<dim3(F / 16, S / 16), 256, 0, stream>>>(xn, w1, hh, D, F, b1, 1);
  k_attn<<<dim3(S / 64, H), 64, 0, stream>>>(qkv, o);
  k_gemm<<<dim3(D / 16, S / 16), 256, 0, stream>>>(o, wo, s, D, D, nullptr, 0);
  k_gemm<<<dim3(D / 16, S / 16), 256, 0, stream>>>(hh, w2, g, F, D, b2, 2);
  k_final<<<S * D / 256, 256, 0, stream>>>(x, s, g, (__hip_bfloat16*)d_out);
}
```

```cpp
#include <hip/hip_runtime.h>
#include <hip/hip_bf16.h>

typedef unsigned short u16;
static constexpr int S = 2048, D = 640, H = 32, DH = 20, F = 1536;

__device__ __forceinline__ float bf2f(u16 v) { return __uint_as_float(((unsigned)v) << 16); }

__global__ void k_ln(const u16* x, float* xn) {
  int row = blockIdx.x;
  __shared__ float red[2][64];
  float s = 0.f;
  for (int i = threadIdx.x; i < D; i += 64) s += bf2f(x[row * D + i]);
  for (int o = 32; o; o >>= 1) s += __shfl_xor(s, o);
  float m = s / D, v = 0.f;
  for (int i = threadIdx.x; i < D; i += 64) { float d = bf2f(x[row * D + i]) - m; v += d * d; }
  for (int o = 32; o; o >>= 1) v += __shfl_xor(v, o);
  float r = rsqrtf(v / D + 1e-5f);
  for (int i = threadIdx.x; i < D; i += 64) xn[row * D + i] = (bf2f(x[row * D + i]) - m) * r;
}

__global__ void k_gemm(const float* a, const u16* w, float* c, int K, int N, const u16* bias, int mode) {
  __shared__ float as[16][17], bs[16][17];
  int tx = threadIdx.x & 15, ty = threadIdx.x >> 4;
  int row = blockIdx.y * 16 + ty, col = blockIdx.x * 16 + tx;
  float acc = 0.f;
  for (int k0 = 0; k0 < K; k0 += 16) {
    as[ty][tx] = a[row * K + k0 + tx];
    bs[ty][tx] = bf2f(w[(k0 + ty) * N + col]);
    __syncthreads();
    for (int k = 0; k < 16; ++k) acc += as[ty][k] * bs[k][tx];
    __syncthreads();
  }
  if (mode >= 1) acc += bf2f(bias[col]);
  if (mode == 1) { acc = fmaxf(acc, 0.f); acc *= acc; }
  c[row * N + col] = acc;
}

__global__ void k_attn(const float* qkv, float* o) {
  int h = blockIdx.y, q = blockIdx.x * 64 + threadIdx.x;
  float qv[DH], acc[DH];
  for (int d = 0; d < DH; ++d) { qv[d] = qkv[q * 3 * D + h * DH + d] * 0.22360679775f; acc[d] = 0.f; }
  float m = -INFINITY, l = 0.f;
  for (int k = 0; k < S; ++k) {
    const float* kp = qkv + k * 3 * D + D + h * DH;
    float s = 0.f;
    for (int d = 0; d < DH; ++d) s += qv[d] * kp[d];
    float mn = fmaxf(m, s), al = __expf(m - mn), p = __expf(s - mn);
    l = l * al + p;
    for (int d = 0; d < DH; ++d) acc[d] = acc[d] * al + p * kp[D + d];
    m = mn;
  }
  for (int d = 0; d < DH; ++d) o[q * D + h * DH + d] = acc[d] / l;
}

__global__ void k_final(const u16* x, const float* s, const float* g, __hip_bfloat16* out) {
  int i = blockIdx.x * 256 + threadIdx.x;
  float gv = g[i];
  out[i] = __float2bfloat16(bf2f(x[i]) + s[i] * gv / (1.f + __expf(-gv)));
}


typedef __attribute__((ext_vector_type(4))) unsigned u4;
__global__ void __launch_bounds__(512) k_p1(char* buf, unsigned long long* stamps, int nstore, int sc1, int spin) {
  extern __shared__ __attribute__((aligned(16))) char smem[];
  unsigned long long t, z;
  asm volatile("s_memrealtime %0\n s_waitcnt lgkmcnt(0)" : "=s"(t) :: "memory");
  do { asm volatile("s_memrealtime %0\n s_waitcnt lgkmcnt(0)" : "=s"(z) :: "memory"); } while (z - t < (unsigned long long)spin);
  char* p = buf + (size_t)blockIdx.x * 65536 + threadIdx.x * 16;
  u4 v = {threadIdx.x, 1u, 2u, 3u};
  for (int i = 0; i < nstore; ++i) {
    if (sc1) asm volatile("global_store_dwordx4 %0, %1, off sc1\n s_nop 1" :: "v"(p + i * 8192), "v"(v) : "memory");
    else asm volatile("global_store_dwordx4 %0, %1, off\n s_nop 1" :: "v"(p + i * 8192), "v"(v) : "memory");
  }
  asm volatile("s_memrealtime %0\n s_waitcnt lgkmcnt(0)" : "=s"(t) :: "memory");
  if (threadIdx.x == 0) stamps[blockIdx.x] = t;
}
__global__ void __launch_bounds__(512) k_p2(const char* buf, unsigned long long* stamps, int what) {
  extern __shared__ __attribute__((aligned(16))) char smem[];
  unsigned long long t;
  asm volatile("s_memrealtime %0\n s_waitcnt lgkmcnt(0)" : "=s"(t) :: "memory");
  if (blockIdx.x == what && threadIdx.x < 64) {
    unsigned long long mx = 0;
    for (int i = 0; i < 256; ++i) { unsigned long long s = stamps[i]; mx = s > mx ? s : mx; }
    unsigned long long dur = t - mx;
    if (dur > 2000ull) dur = 2000ull;
    dur *= 200;
    unsigned long long z;
    do { __builtin_amdgcn_s_sleep(8); asm volatile("s_memrealtime %0\n s_waitcnt lgkmcnt(0)" : "=s"(z) :: "memory"); } while (z - t < dur);
  }
}
__global__ void __launch_bounds__(512) k_t0(unsigned long long* stamps) {
  unsigned long long t;
  asm volatile("s_memrealtime %0\n s_waitcnt lgkmcnt(0)" : "=s"(t) :: "memory");
  if (blockIdx.x == 0 && threadIdx.x == 0) stamps[0] = t;
}
__global__ void __launch_bounds__(512) k_t1(const unsigned long long* stamps) {
  unsigned long long t = stamps[0], z, a;
  asm volatile("s_memrealtime %0\n s_waitcnt lgkmcnt(0)" : "=s"(a) :: "memory");
  do { asm volatile("s_memrealtime %0\n s_waitcnt lgkmcnt(0)" : "=s"(z) :: "memory"); } while (z - t < 250000ull && z - a < 250000ull);
}

extern "C" void kernel_launch(void* const* d_in, const int* in_sizes, int n_in, void* d_out, int out_size,
                              void* d_ws, size_t ws_size, hipStream_t stream) {
  const u16 *x = (const u16*)d_in[0], *wqkv = (const u16*)d_in[1], *wo = (const u16*)d_in[2];
  const u16 *w1 = (const u16*)d_in[4], *b1 = (const u16*)d_in[5], *w2 = (const u16*)d_in[6], *b2 = (const u16*)d_in[7];
  float* ws = (float*)d_ws;
  float *xn = ws, *qkv = xn + S * D, *hh = qkv + S * 3 * D, *o = hh + S * F, *s = o + S * D, *g = s + S * D;
  hipFuncSetAttribute((const void*)k_p1, hipFuncAttributeMaxDynamicSharedMemorySize, 147456);
  hipFuncSetAttribute((const void*)k_p2, hipFuncAttributeMaxDynamicSharedMemorySize, 147456);
  char* pb = (char*)d_ws + (128 << 20); unsigned long long* st = (unsigned long long*)((char*)d_ws + (200 << 20));
  k_p1<<<256, 512, 147456, stream>>>(pb, st, 0, 0, 1000);
  k_p2<<<256, 512, 147456, stream>>>(pb, st, 200);
  k_ln<<<S, 64, 0, stream>>>(x, xn);
  k_gemm<<<dim3(3 * D / 16, S / 16), 256, 0, stream>>>(xn, wqkv, qkv, D, 3 * D, nullptr, 0);
  k_gemm<<<dim3(F / 16, S / 16), 256, 0, stream>>>(xn, w1, hh, D, F, b1, 1);
  k_attn<<<dim3(S / 64, H), 64, 0, stream>>>(qkv, o);
  k_gemm<<<dim3(D / 16, S / 16), 256, 0, stream>>>(o, wo, s, D, D, nullptr, 0);
  k_gemm<<<dim3(D / 16, S / 16), 256, 0, stream>>>(hh, w2, g, F, D, b2, 2);
  k_final<<<S * D / 256, 256, 0, stream>>>(x, s, g, (__hip_bfloat16*)d_out);
}
```

```cpp
#include <hip/hip_runtime.h>
#include <hip/hip_bf16.h>

typedef unsigned short u16;
typedef unsigned u32;
typedef unsigned long long u64;
typedef __attribute__((ext_vector_type(2))) u32 u32x2;
typedef __attribute__((ext_vector_type(4))) u32 u32x4;
typedef __attribute__((ext_vector_type(8))) u32 u32x8;
typedef __attribute__((ext_vector_type(2))) u64 u64x2;
typedef __attribute__((ext_vector_type(2))) float f32x2;
typedef __attribute__((ext_vector_type(4))) float f32x4;
typedef __attribute__((ext_vector_type(8), aligned(16))) float f32x8;
typedef __attribute__((ext_vector_type(16))) float f32x16;
typedef __attribute__((ext_vector_type(32))) float f32x32;
typedef __attribute__((ext_vector_type(4))) short s16x4;
typedef __attribute__((ext_vector_type(2))) __bf16 bf16x2;
typedef __attribute__((ext_vector_type(8))) __bf16 bf16x8;
typedef __attribute__((ext_vector_type(16))) __bf16 bf16x16;
#define LAS __attribute__((address_space(3)))
#define CAS __attribute__((address_space(4)))
#define GAS __attribute__((address_space(1)))
typedef GAS const char* gptr;
#if defined(__HIP_DEVICE_COMPILE__)
#define NOPK __attribute__((target("no-packed-fp32-ops")))
#else
#define NOPK
#endif
#define DEV __device__ __forceinline__ NOPK
#define LAM __attribute__((always_inline)) NOPK
#define SC1 16

constexpr u32 WS_QP = 0;
constexpr u32 WS_KP = 2621440;
constexpr u32 WS_VT = 5242880;
constexpr u32 WS_HP = 7864320;
constexpr u32 WS_W2F = 14155776;
constexpr u32 WS_WOF = 16121856;
constexpr u32 WS_O = 16941056;
constexpr u32 WS_FLAGS = 19562496;
constexpr u32 WS_ST = WS_FLAGS + 4096;
constexpr u32 WS_FQ = WS_ST + 49152;
constexpr u32 WS_FH = WS_FQ + 65536;
constexpr u32 WS_END = WS_FH + 65536;
constexpr u32 WS_DBG = WS_END;

constexpr u32 L_W = 98304, L_ST = 159744, L_CP = 49152, L_H = 115200, L_CV = 139776, L_PRE = 23040, L_Q = 32768, L_SY = 163584;
constexpr u32 SY_G2 = L_SY, SY_AD = L_SY + 32, SY_RD = L_SY + 64;
#define XCH(a) ((a) < 7 ? KV_SLOT + 3072 * (a) : 158208)
#define QOFF(w) (3072 * (w))
constexpr u32 KV_SLOT = 23040, KV_V = 12288;
constexpr u32 L_WOP = 46080;
constexpr u32 SPIN_LDS = 1u << 22, SPIN_GLB = 1u << 19;

#define STAMP(k)

DEV void bar() {
  __builtin_amdgcn_sched_barrier(0);
  asm volatile("s_waitcnt lgkmcnt(0)\n s_barrier" ::: "memory");
  __builtin_amdgcn_sched_barrier(0);
}
template <int N> DEV void vmwait() { asm volatile("s_waitcnt vmcnt(%0)" :: "n"(N) : "memory"); }
DEV void dma1_sc1(u32 a, gptr base, u32 dst) {
  asm volatile("s_mov_b32 m0, %2\n s_nop 0\n global_load_lds_dwordx4 %0, %1 sc1" :: "v"(a), "s"(base), "s"(dst) : "memory");
}
template <class T> DEV T tmin(T a, T b) { return a < b ? a : b; }
template <class T> DEV T tmax(T a, T b) { return a > b ? a : b; }
template <int C> DEV float dpp(float v) { return __builtin_bit_cast(float, __builtin_amdgcn_update_dpp(0, __builtin_bit_cast(int, v), C, 15, 15, true)); }
DEV float sum32(float v) {
  v += dpp<0xB1>(v); v += dpp<0x4E>(v); v += dpp<0x141>(v); v += dpp<0x140>(v);
  const u32 u = __builtin_bit_cast(u32, v);
  const auto r = __builtin_amdgcn_permlane16_swap(u, u, false, false);
  return __builtin_bit_cast(float, (u32)r[0]) + __builtin_bit_cast(float, (u32)r[1]);
}
DEV f32x2 halves(float v) {
  const u32 u = __builtin_bit_cast(u32, v);
  const auto r = __builtin_amdgcn_permlane32_swap(u, u, false, false);
  return (f32x2){__builtin_bit_cast(float, (u32)r[0]), __builtin_bit_cast(float, (u32)r[1])};
}
DEV u32 pk(float a, float b) { f32x2 v = {a, b}; return __builtin_bit_cast(u32, __builtin_convertvector(v, bf16x2)); }
DEV u32 sel(bool c, u32 a, u32 b) { asm("" : "+v"(a), "+v"(b)); return c ? a : b; }
DEV float asf(u32 v) { return __builtin_bit_cast(float, v); }
DEV float lo16(u32 v) { return __builtin_bit_cast(float, v << 16); }
DEV float hi16(u32 v) { return __builtin_bit_cast(float, v & 0xffff0000u); }
DEV u32 tau(u32 i) { return (i & ~12u) | ((i & 4u) << 1) | ((i & 8u) >> 1); }
DEV bf16x8 asfrag(u32x4 v) { return __builtin_bit_cast(bf16x8, v); }
DEV f32x16 mm32(bf16x8 a, bf16x8 b, f32x16 c) { return __builtin_amdgcn_mfma_f32_32x32x16_bf16(a, b, c, 0, 0, 0); }
DEV bf16x8 trfrag(LAS char* p, u32 second) {
  s16x4 a = __builtin_amdgcn_ds_read_tr16_b64_v4i16((LAS s16x4*)p);
  s16x4 b = __builtin_amdgcn_ds_read_tr16_b64_v4i16((LAS s16x4*)(p + second));
  return __builtin_bit_cast(bf16x8, __builtin_shufflevector(a, b, 0, 1, 2, 3, 4, 5, 6, 7));
}
DEV void st128(u32x4 v, u32x4 srd, u32 voff, u32 soff) {
  asm volatile("buffer_store_dwordx4 %0, %1, %2, %3 offen sc1\n s_nop 1" :: "v"(v), "v"(voff), "s"(srd), "s"(soff) : "memory");
}
DEV void dma_ready(gptr src, u32 dst) {
  asm volatile("s_mov_b32 m0, %2\n s_mov_b64 exec, 1\n global_load_lds_dword %0, %1 sc1\n s_mov_b64 exec, -1" :: "v"(0u), "s"(src), "s"(dst) : "memory");
}
DEV void kv_k(u32 a, u32 b, u32 c, gptr base, u32 dst) {
  asm volatile("s_mov_b32 m0, %4\n s_nop 0\n global_load_lds_dwordx4 %0, %3 sc1\n global_load_lds_dwordx4 %1, %3 offset:1024 sc1\n"
               "global_load_lds_dwordx4 %2, %3 offset:2048 sc1" :: "v"(a), "v"(b), "v"(c), "s"(base), "s"(dst) : "memory");
}
DEV void kv_v(u32 a, u32 b, u32 c, gptr base, u32 dst) {
  asm volatile("s_mov_b32 m0, %4\n s_nop 0\n global_load_lds_dwordx4 %0, %3 sc1\n global_load_lds_dwordx4 %1, %3 offset:1024 sc1\n"
               "s_mov_b32 exec_hi, 0\n global_load_lds_dwordx4 %2, %3 offset:2048 sc1\n s_mov_b32 exec_hi, -1"
               :: "v"(a), "v"(b), "v"(c), "s"(base), "s"(dst) : "memory");
}
DEV void lds_post(LAS char* L, u32 off, u32 v) {
  asm volatile("s_waitcnt lgkmcnt(0)\n ds_write_b32 %0, %1" :: "v"((u32)(size_t)(L + off)), "v"(v) : "memory");
}
DEV void lds_wait(LAS char* L, u32 off, u32 need) {
  u32 v, n;
  asm volatile("s_mov_b32 %1, %4\n"
               "1:\n ds_read_b32 %0, %2\n s_waitcnt lgkmcnt(0)\n v_cmp_gt_u32 vcc, %3, %0\n s_cbranch_vccz 2f\n"
               "s_sleep 1\n s_sub_u32 %1, %1, 1\n s_cbranch_scc0 1b\n 2:"
               : "=&v"(v), "=&s"(n) : "v"((u32)(size_t)(L + off)), "s"(need), "n"(SPIN_LDS) : "vcc", "scc", "memory");
}
DEV u32 fresh_lane() {
  u32 l;
  asm volatile("v_mbcnt_lo_u32_b32 %0, -1, 0\n v_mbcnt_hi_u32_b32 %0, -1, %0" : "=v"(l));
  __builtin_assume(l < 64);
  return l;
}
#define LANEVARS const u32 lane = fresh_lane(), hh = lane >> 5, tok = lane & 31, g16 = (lane >> 4) & 1, q4 = (lane >> 2) & 3, p4 = lane & 3; \
  (void)hh; (void)tok; (void)g16; (void)q4; (void)p4;

#define CVT_GEOMETRY(unit) \
  const u32 cu = unit, cstep = cu >> 3, ccg = cu & 7; \
  const bool isw2 = cstep < 48; \
  const u32 os = isw2 ? 0 : cstep - 48; \
  const u32 oq = (os * 52) >> 8, oi = os - 5 * oq; \
  const u32 crow = isw2 ? 32 * cstep : 160 * oq + (oi < 4 ? 40 * oi : 16); \
  const u32 e8 = oi == 4 ? 32 : 0, e16 = !isw2 && oi < 4 ? 8 : 0; \
  const gptr cvb = (isw2 ? w2 : wo) + crow * 1280 + ccg * 160;

DEV f32x16 attn_exact(__amdgpu_buffer_rsrc_t rs, u32 qbase, u32 qm, u32 qr, u32 kr, u32 vo, bool vrow) {
  const bf16x8 qf = asfrag(__builtin_amdgcn_raw_buffer_load_b128(rs, qbase + qm, 0, SC1));
  const s16x4 qrf = __builtin_bit_cast(s16x4, __builtin_amdgcn_raw_buffer_load_b64(rs, qbase + qr, 0, SC1));
  float m = -__builtin_inff();
  f32x16 o = {};
#pragma unroll 1
  for (int it = 0; it < 64; ++it) {
    const u32 kb = WS_KP + (it & 31) * 81920;
    const bf16x8 k0 = asfrag(__builtin_amdgcn_raw_buffer_load_b128(rs, kb + qm, 0, SC1));
    const bf16x8 k1 = asfrag(__builtin_amdgcn_raw_buffer_load_b128(rs, kb + 40960 + qm, 0, SC1));
    const s16x4 krf = __builtin_bit_cast(s16x4, __builtin_amdgcn_raw_buffer_load_b64(rs, kb + kr, 0, SC1));
    f32x32 s = {};
    s = __builtin_amdgcn_mfma_f32_32x32x4bf16_1k(krf, qrf, s, 0, 0, 0);
    f32x16 s0 = mm32(k0, qf, __builtin_shufflevector(s, s, 0, 1, 2, 3, 4, 5, 6, 7, 8, 9, 10, 11, 12, 13, 14, 15));
    f32x16 s1 = mm32(k1, qf, __builtin_shufflevector(s, s, 16, 17, 18, 19, 20, 21, 22, 23, 24, 25, 26, 27, 28, 29, 30, 31));
    if (it < 32) {
#pragma unroll
      for (int j = 0; j < 16; ++j) m = __builtin_fmaxf(m, __builtin_fmaxf(s0[j], s1[j]));
      if (it == 31) { const f32x2 hm = halves(m); m = __builtin_fmaxf(hm.x, hm.y); }
    } else {
#pragma unroll
      for (int i = 0; i < 4; ++i) {
        u32x4 v = __builtin_amdgcn_raw_buffer_load_b128(rs, WS_VT + ((it & 31) * 4 + i) * 20480 + vo, 0, SC1);
        if (!vrow) v = (u32x4){0x3f803f80u, 0x3f803f80u, 0x3f803f80u, 0x3f803f80u};
        f32x8 pr;
#pragma unroll
        for (int j = 0; j < 8; ++j) pr[j] = __builtin_amdgcn_exp2f((i < 2 ? s0 : s1)[(i & 1) * 8 + j] - m);
        o = __builtin_amdgcn_smfmac_f32_32x32x32_bf16(asfrag(v), __builtin_bit_cast(bf16x16, pr), o, (int)0xdddddddd, 0, 0);
      }
    }
  }
  return o;
}

__global__ void __launch_bounds__(768, 3) NOPK
fused_block(const char* a_x, const char* a_wqkv, const char* a_wo, const char* a_w1, const char* a_b1, const char* a_w2, const char* a_b2,
            char* a_out, char* a_ws) {
  asm volatile("s_load_dword s94, %0, 0\n s_load_dword s95, %0, 64" :: "s"(__builtin_amdgcn_kernarg_segment_ptr()) : "s94", "s95");
  __builtin_amdgcn_sched_barrier(0);
  __shared__ __attribute__((aligned(16))) char smem[163840];
  LAS char* L = (LAS char*)smem;
  const u32 lbase = (u32)(size_t)L;
  const u32 hw = __builtin_amdgcn_readfirstlane(__builtin_amdgcn_workitem_id_x() >> 6), wave = hw < 4 ? hw + 8 : hw - 4, grp = wave >> 2, w = wave & 3;
  const u32 b = __builtin_amdgcn_workgroup_id_x(), xcd = b & 7, bi = (b >> 3) & 31;
  const u32 rb = 8 * (xcd >> 2) + (bi & 7), cb = 4 * (xcd & 3) + (bi >> 3);
  const u32 qc = !(xcd & 1) ? (xcd & 2) * 2 + (bi >> 3) : bi < 24 ? 8 + 3 * ((xcd >> 1) & 1) + (bi >> 3) : 14 + (((bi >> 2) ^ (xcd >> 1)) & 1);
  const u32 head = 4 * xcd + (bi & 3), qb = bi >> 2;
  const u32 rt = 4 * xcd + (bi & 3), cg = bi >> 2;
  STAMP(0);
  const u32 odd = head & 1, plA = 5 * (head >> 1) + 3 * odd, plR = 5 * (head >> 1) + 2;
  auto vcol = [&](u32 r) LAM { return 20 * head + sel(r < 16, tau(r) + 4 * odd, (r & 3) + 16 * (1 - odd)); };
  const u32 k0 = ((xcd & 3) * 5 / 2 + 5 * (xcd >> 2) + (bi & 1)) % 10, wst = 245760, lw = lbase + w * 4096, xoff = rb * 163840, woff = tmin(qc, 14u) * 256, wm = qc < 15 ? ~0u : 0u;
  if (grp == 2) {
    asm volatile(
      "s_load_dwordx4 s[60:63], %[ka], 0\n"
      "v_mbcnt_lo_u32_b32 v8, -1, 0\n"
      "v_mbcnt_hi_u32_b32 v8, -1, v8\n"
      "v_lshrrev_b32 v9, 3, v8\n"
      "v_lshrrev_b32 v10, 4, v8\n"
      "v_lshl_or_b32 v9, %[w], 5, v9\n"
      "v_and_b32 v11, 7, v8\n"
      "v_xor_b32 v11, v11, v10\n"
      "v_mul_u32_u24 v9, 1280, v9\n"
      "v_lshl_or_b32 v12, v11, 4, v9\n"
      "v_xor_b32 v15, 64, v12\n"
      "v_add_u32 v13, 9216, v15\n"
      "v_add_u32 v14, 18432, v12\n"
      "v_add_u32 v15, 27648, v15\n"
      "s_add_u32 m0, %[lw], 65536\n"
      "v_lshl_or_b32 v9, %[w], 4, v10\n"
      "v_mul_u32_u24 v9, 3840, v9\n"
      "v_and_b32 v11, 15, v8\n"
      "v_lshlrev_b32 v10, 2, v10\n"
      "v_xor_b32 v11, v11, v10\n"
      "v_lshl_or_b32 v16, v11, 4, v9\n"
      "v_and_b32 v16, %[wm], v16\n"
      "v_add_u32 v17, 14336, v16\n"
      "v_add_u32 v18, 28672, v16\n"
      "v_add_u32 v19, 43008, v16\n"
      "s_waitcnt lgkmcnt(0)\n"
      "s_add_u32 s60, s60, %[xk]\n"
      "s_addc_u32 s61, s61, 0\n"
      "global_load_lds_dwordx4 v12, s[60:61]\n"
      "global_load_lds_dwordx4 v13, s[60:61] offset:1024\n"
      "global_load_lds_dwordx4 v14, s[60:61] offset:2048\n"
      "global_load_lds_dwordx4 v15, s[60:61] offset:3072\n"
      "s_add_u32 m0, %[lw], 98304\n"
      "s_add_u32 s62, s62, %[wk]\n"
      "s_addc_u32 s63, s63, 0\n"
      "global_load_lds_dwordx4 v16, s[62:63]\n"
      "global_load_lds_dwordx4 v17, s[62:63] offset:1024\n"
      "global_load_lds_dwordx4 v18, s[62:63] offset:2048\n"
      "global_load_lds_dwordx4 v19, s[62:63] offset:3072\n"
      :: [ka] "s"(__builtin_amdgcn_kernarg_segment_ptr()), [w] "s"(w), [lw] "s"(lw), [wm] "s"(wm), [xk] "s"(xoff + k0 * 128), [wk] "s"(woff + k0 * wst)
      : "v8", "v9", "v10", "v11", "v12", "v13", "v14", "v15", "v16", "v17", "v18", "v19", "s60", "s61", "s62", "s63", "vcc", "scc", "memory");
  }
  if (1 && wave == 0) {
    asm volatile("s_getpc_b64 s[60:61]\n v_mbcnt_lo_u32_b32 v8, -1, 0\n v_lshlrev_b32 v8, 12, v8\n v_add_u32 v8, %0, v8\n v_min_u32 v8, %1, v8\n"
                 "s_mov_b64 exec, 0x1ff\n global_load_dword v167, v8, s[60:61]\n s_mov_b64 exec, -1"
                 :: "s"(bi * 128), "n"(33000) : "s60", "s61", "v8", "v167", "memory");
  }
  const gptr x = (gptr)a_x, wqkv = (gptr)a_wqkv, wo = (gptr)a_wo, w1 = (gptr)a_w1, b1 = (gptr)a_b1, w2 = (gptr)a_w2, b2 = (gptr)a_b2;
  const gptr out = (gptr)a_out, ws = (gptr)a_ws;
  asm volatile("" :: "s"(x), "s"(wqkv), "s"(wo), "s"(w1), "s"(b1), "s"(w2), "s"(b2), "s"(out), "s"(ws));
  const u64 kw = (u64)ws;
  const __amdgpu_buffer_rsrc_t rs = __builtin_amdgcn_make_buffer_rsrc(a_ws, 0, WS_END, 0x00020000);
  const u32x4 srd = {(u32)kw, (u32)(kw >> 32) & 0xffffu, WS_END, 0x00020000u};
  const CAS u64* epoch = (const CAS u64*)(kw + WS_FLAGS);

  if (grp == 2) {
    const u32 ln = fresh_lane();
    const u32 x0 = (32 * w + (ln >> 3)) * 1280 + (((ln & 7) ^ (ln >> 4)) << 4), x1 = (x0 ^ 64) + 9216, x2 = x0 + 18432, x3 = (x0 ^ 64) + 27648;
    const u32 w0 = ((16 * w + (ln >> 4)) * 3840 + (((ln & 15) ^ ((ln >> 4) << 2)) << 4)) & wm;
    const u32 wo1 = w0 + 14336, wo2 = w0 + 2 * 14336, wo3 = w0 + 3 * 14336;
    STAMP(1);
    {
      u32 uo[3];
#pragma unroll
      for (u32 p = 0; p < 3; ++p) {
        const u32 by = 1024 * p + 16 * ln, r = by / 192, o = by - r * 192;
        uo[p] = (16 * w + r) * 3072 + o - 1024 * p;
      }

      asm volatile(
      "s_mov_b64 s[76:77], %[xb]\n"
      "s_mov_b64 s[78:79], %[wb]\n"
      "s_mov_b64 s[74:75], %[ub]\n"
      "s_add_u32 s68, %[k0], 1\n"
      "s_cmp_ge_u32 s68, 10\n"
      "s_cselect_b32 s69, 10, 0\n"
      "s_sub_u32 s68, s68, s69\n"
      "s_lshl_b32 s69, s68, 7\n"
      "s_add_u32 s60, s76, s69\n"
      "s_addc_u32 s61, s77, 0\n"
      "s_mul_i32 s69, s68, 245760\n"
      "s_add_u32 s62, s78, s69\n"
      "s_addc_u32 s63, s79, 0\n"
      "s_add_u32 m0, %[lw], 81920\n"
      "s_nop 0\n"
      "global_load_lds_dwordx4 %[x0], s[60:61]\n"
      "global_load_lds_dwordx4 %[x1], s[60:61] offset:1024\n"
      "global_load_lds_dwordx4 %[x2], s[60:61] offset:2048\n"
      "global_load_lds_dwordx4 %[x3], s[60:61] offset:3072\n"
      "s_add_u32 m0, %[lw], 114688\n"
      "s_nop 0\n"
      "global_load_lds_dwordx4 %[w0], s[62:63]\n"
      "global_load_lds_dwordx4 %[w1], s[62:63] offset:1024\n"
      "global_load_lds_dwordx4 %[w2], s[62:63] offset:2048\n"
      "global_load_lds_dwordx4 %[w3], s[62:63] offset:3072\n"
      "s_add_u32 s68, %[k0], 0\n"
      "s_cmp_ge_u32 s68, 10\n"
      "s_cselect_b32 s69, 10, 0\n"
      "s_sub_u32 s68, s68, s69\n"
      "s_mul_i32 s69, s68, 196608\n"
      "s_add_u32 s62, s74, s69\n"
      "s_addc_u32 s63, s75, 0\n"
      "s_add_u32 m0, %[l3], 32768\n"
      "s_waitcnt vmcnt(8)\n"
      "s_barrier\n"
      "global_load_lds_dwordx4 %[u0], s[62:63]\n"
      "global_load_lds_dwordx4 %[u1], s[62:63] offset:1024\n"
      "global_load_lds_dwordx4 %[u2], s[62:63] offset:2048\n"
      "s_add_u32 s68, %[k0], 2\n"
      "s_cmp_ge_u32 s68, 10\n"
      "s_cselect_b32 s69, 10, 0\n"
      "s_sub_u32 s68, s68, s69\n"
      "s_lshl_b32 s69, s68, 7\n"
      "s_add_u32 s60, s76, s69\n"
      "s_addc_u32 s61, s77, 0\n"
      "s_mul_i32 s69, s68, 245760\n"
      "s_add_u32 s62, s78, s69\n"
      "s_addc_u32 s63, s79, 0\n"
      "s_add_u32 m0, %[lw], 0\n"
      "s_nop 0\n"
      "global_load_lds_dwordx4 %[x0], s[60:61]\n"
      "global_load_lds_dwordx4 %[x1], s[60:61] offset:1024\n"
      "global_load_lds_dwordx4 %[x2], s[60:61] offset:2048\n"
      "global_load_lds_dwordx4 %[x3], s[60:61] offset:3072\n"
      "s_add_u32 m0, %[lw], 131072\n"
      "s_nop 0\n"
      "global_load_lds_dwordx4 %[w0], s[62:63]\n"
      "global_load_lds_dwordx4 %[w1], s[62:63] offset:1024\n"
      "global_load_lds_dwordx4 %[w2], s[62:63] offset:2048\n"
      "global_load_lds_dwordx4 %[w3], s[62:63] offset:3072\n"
      "s_add_u32 s68, %[k0], 1\n"
      "s_cmp_ge_u32 s68, 10\n"
      "s_cselect_b32 s69, 10, 0\n"
      "s_sub_u32 s68, s68, s69\n"
      "s_mul_i32 s69, s68, 196608\n"
      "s_add_u32 s62, s74, s69\n"
      "s_addc_u32 s63, s75, 0\n"
      "s_add_u32 m0, %[l3], 49152\n"
      "s_waitcnt vmcnt(8)\n"
      "s_barrier\n"
      "global_load_lds_dwordx4 %[u0], s[62:63]\n"
      "global_load_lds_dwordx4 %[u1], s[62:63] offset:1024\n"
      "global_load_lds_dwordx4 %[u2], s[62:63] offset:2048\n"
      "s_add_u32 s68, %[k0], 3\n"
      "s_cmp_ge_u32 s68, 10\n"
      "s_cselect_b32 s69, 10, 0\n"
      "s_sub_u32 s68, s68, s69\n"
      "s_lshl_b32 s69, s68, 7\n"
      "s_add_u32 s60, s76, s69\n"
      "s_addc_u32 s61, s77, 0\n"
      "s_mul_i32 s69, s68, 245760\n"
      "s_add_u32 s62, s78, s69\n"
      "s_addc_u32 s63, s79, 0\n"
      "s_add_u32 m0, %[lw], 16384\n"
      "s_nop 0\n"
      "global_load_lds_dwordx4 %[x0], s[60:61]\n"
      "global_load_lds_dwordx4 %[x1], s[60:61] offset:1024\n"
      "global_load_lds_dwordx4 %[x2], s[60:61] offset:2048\n"
      "global_load_lds_dwordx4 %[x3], s[60:61] offset:3072\n"
      "s_add_u32 m0, %[lw], 98304\n"
      "s_nop 0\n"
      "global_load_lds_dwordx4 %[w0], s[62:63]\n"
      "global_load_lds_dwordx4 %[w1], s[62:63] offset:1024\n"
      "global_load_lds_dwordx4 %[w2], s[62:63] offset:2048\n"
      "global_load_lds_dwordx4 %[w3], s[62:63] offset:3072\n"
      "s_add_u32 s68, %[k0], 2\n"
      "s_cmp_ge_u32 s68, 10\n"
      "s_cselect_b32 s69, 10, 0\n"
      "s_sub_u32 s68, s68, s69\n"
      "s_mul_i32 s69, s68, 196608\n"
      "s_add_u32 s62, s74, s69\n"
      "s_addc_u32 s63, s75, 0\n"
      "s_add_u32 m0, %[l3], 147456\n"
      "s_waitcnt vmcnt(8)\n"
      "s_barrier\n"
      "global_load_lds_dwordx4 %[u0], s[62:63]\n"
      "global_load_lds_dwordx4 %[u1], s[62:63] offset:1024\n"
      "global_load_lds_dwordx4 %[u2], s[62:63] offset:2048\n"
      "s_add_u32 s68, %[k0], 4\n"
      "s_cmp_ge_u32 s68, 10\n"
      "s_cselect_b32 s69, 10, 0\n"
      "s_sub_u32 s68, s68, s69\n"
      "s_lshl_b32 s69, s68, 7\n"
      "s_add_u32 s60, s76, s69\n"
      "s_addc_u32 s61, s77, 0\n"
      "s_mul_i32 s69, s68, 245760\n"
      "s_add_u32 s62, s78, s69\n"
      "s_addc_u32 s63, s79, 0\n"
      "s_add_u32 m0, %[lw], 32768\n"
      "s_nop 0\n"
      "global_load_lds_dwordx4 %[x0], s[60:61]\n"
      "global_load_lds_dwordx4 %[x1], s[60:61] offset:1024\n"
      "global_load_lds_dwordx4 %[x2], s[60:61] offset:2048\n"
      "global_load_lds_dwordx4 %[x3], s[60:61] offset:3072\n"
      "s_add_u32 m0, %[lw], 114688\n"
      "s_nop 0\n"
      "global_load_lds_dwordx4 %[w0], s[62:63]\n"
      "global_load_lds_dwordx4 %[w1], s[62:63] offset:1024\n"
      "global_load_lds_dwordx4 %[w2], s[62:63] offset:2048\n"
      "global_load_lds_dwordx4 %[w3], s[62:63] offset:3072\n"
      "s_add_u32 s68, %[k0], 5\n"
      "s_cmp_ge_u32 s68, 10\n"
      "s_cselect_b32 s69, 10, 0\n"
      "s_sub_u32 s68, s68, s69\n"
      "s_lshl_b32 s69, s68, 7\n"
      "s_add_u32 s60, s76, s69\n"
      "s_addc_u32 s61, s77, 0\n"
      "s_mul_i32 s69, s68, 245760\n"
      "s_add_u32 s62, s78, s69\n"
      "s_addc_u32 s63, s79, 0\n"
      "s_add_u32 m0, %[lw], 49152\n"
      "s_waitcnt vmcnt(8)\n"
      "s_barrier\n"
      "global_load_lds_dwordx4 %[x0], s[60:61]\n"
      "global_load_lds_dwordx4 %[x1], s[60:61] offset:1024\n"
      "global_load_lds_dwordx4 %[x2], s[60:61] offset:2048\n"
      "global_load_lds_dwordx4 %[x3], s[60:61] offset:3072\n"
      "s_add_u32 m0, %[lw], 131072\n"
      "s_nop 0\n"
      "global_load_lds_dwordx4 %[w0], s[62:63]\n"
      "global_load_lds_dwordx4 %[w1], s[62:63] offset:1024\n"
      "global_load_lds_dwordx4 %[w2], s[62:63] offset:2048\n"
      "global_load_lds_dwordx4 %[w3], s[62:63] offset:3072\n"
      "s_add_u32 s68, %[k0], 6\n"
      "s_cmp_ge_u32 s68, 10\n"
      "s_cselect_b32 s69, 10, 0\n"
      "s_sub_u32 s68, s68, s69\n"
      "s_lshl_b32 s69, s68, 7\n"
      "s_add_u32 s60, s76, s69\n"
      "s_addc_u32 s61, s77, 0\n"
      "s_mul_i32 s69, s68, 245760\n"
      "s_add_u32 s62, s78, s69\n"
      "s_addc_u32 s63, s79, 0\n"
      "s_add_u32 m0, %[lw], 65536\n"
      "s_waitcnt vmcnt(8)\n"
      "s_barrier\n"
      "global_load_lds_dwordx4 %[x0], s[60:61]\n"
      "global_load_lds_dwordx4 %[x1], s[60:61] offset:1024\n"
      "global_load_lds_dwordx4 %[x2], s[60:61] offset:2048\n"
      "global_load_lds_dwordx4 %[x3], s[60:61] offset:3072\n"
      "s_add_u32 m0, %[lw], 98304\n"
      "s_nop 0\n"
      "global_load_lds_dwordx4 %[w0], s[62:63]\n"
      "global_load_lds_dwordx4 %[w1], s[62:63] offset:1024\n"
      "global_load_lds_dwordx4 %[w2], s[62:63] offset:2048\n"
      "global_load_lds_dwordx4 %[w3], s[62:63] offset:3072\n"
      "s_add_u32 s68, %[k0], 7\n"
      "s_cmp_ge_u32 s68, 10\n"
      "s_cselect_b32 s69, 10, 0\n"
      "s_sub_u32 s68, s68, s69\n"
      "s_lshl_b32 s69, s68, 7\n"
      "s_add_u32 s60, s76, s69\n"
      "s_addc_u32 s61, s77, 0\n"
      "s_mul_i32 s69, s68, 245760\n"
      "s_add_u32 s62, s78, s69\n"
      "s_addc_u32 s63, s79, 0\n"
      "s_add_u32 m0, %[lw], 81920\n"
      "s_waitcnt vmcnt(8)\n"
      "s_barrier\n"
      "global_load_lds_dwordx4 %[x0], s[60:61]\n"
      "global_load_lds_dwordx4 %[x1], s[60:61] offset:1024\n"
      "global_load_lds_dwordx4 %[x2], s[60:61] offset:2048\n"
      "global_load_lds_dwordx4 %[x3], s[60:61] offset:3072\n"
      "s_add_u32 m0, %[lw], 114688\n"
      "s_nop 0\n"
      "global_load_lds_dwordx4 %[w0], s[62:63]\n"
      "global_load_lds_dwordx4 %[w1], s[62:63] offset:1024\n"
      "global_load_lds_dwordx4 %[w2], s[62:63] offset:2048\n"
      "global_load_lds_dwordx4 %[w3], s[62:63] offset:3072\n"
      "s_add_u32 s68, %[k0], 8\n"
      "s_cmp_ge_u32 s68, 10\n"
      "s_cselect_b32 s69, 10, 0\n"
      "s_sub_u32 s68, s68, s69\n"
      "s_lshl_b32 s69, s68, 7\n"
      "s_add_u32 s60, s76, s69\n"
      "s_addc_u32 s61, s77, 0\n"
      "s_mul_i32 s69, s68, 245760\n"
      "s_add_u32 s62, s78, s69\n"
      "s_addc_u32 s63, s79, 0\n"
      "s_add_u32 m0, %[lw], 0\n"
      "s_waitcnt vmcnt(8)\n"
      "s_barrier\n"
      "global_load_lds_dwordx4 %[x0], s[60:61]\n"
      "global_load_lds_dwordx4 %[x1], s[60:61] offset:1024\n"
      "global_load_lds_dwordx4 %[x2], s[60:61] offset:2048\n"
      "global_load_lds_dwordx4 %[x3], s[60:61] offset:3072\n"
      "s_add_u32 m0, %[lw], 131072\n"
      "s_nop 0\n"
      "global_load_lds_dwordx4 %[w0], s[62:63]\n"
      "global_load_lds_dwordx4 %[w1], s[62:63] offset:1024\n"
      "global_load_lds_dwordx4 %[w2], s[62:63] offset:2048\n"
      "global_load_lds_dwordx4 %[w3], s[62:63] offset:3072\n"
      "s_add_u32 s68, %[k0], 9\n"
      "s_cmp_ge_u32 s68, 10\n"
      "s_cselect_b32 s69, 10, 0\n"
      "s_sub_u32 s68, s68, s69\n"
      "s_lshl_b32 s69, s68, 7\n"
      "s_add_u32 s60, s76, s69\n"
      "s_addc_u32 s61, s77, 0\n"
      "s_mul_i32 s69, s68, 245760\n"
      "s_add_u32 s62, s78, s69\n"
      "s_addc_u32 s63, s79, 0\n"
      "s_add_u32 m0, %[lw], 16384\n"
      "s_waitcnt vmcnt(8)\n"
      "s_barrier\n"
      "global_load_lds_dwordx4 %[x0], s[60:61]\n"
      "global_load_lds_dwordx4 %[x1], s[60:61] offset:1024\n"
      "global_load_lds_dwordx4 %[x2], s[60:61] offset:2048\n"
      "global_load_lds_dwordx4 %[x3], s[60:61] offset:3072\n"
      "s_add_u32 m0, %[lw], 98304\n"
      "s_nop 0\n"
      "global_load_lds_dwordx4 %[w0], s[62:63]\n"
      "global_load_lds_dwordx4 %[w1], s[62:63] offset:1024\n"
      "global_load_lds_dwordx4 %[w2], s[62:63] offset:2048\n"
      "global_load_lds_dwordx4 %[w3], s[62:63] offset:3072\n"
      "s_add_u32 s68, %[k0], 4\n"
      "s_cmp_ge_u32 s68, 10\n"
      "s_cselect_b32 s69, 10, 0\n"
      "s_sub_u32 s68, s68, s69\n"
      "s_mul_i32 s69, s68, 196608\n"
      "s_add_u32 s62, s74, s69\n"
      "s_addc_u32 s63, s75, 0\n"
      "s_add_u32 m0, %[l3], 147456\n"
      "s_nop 0\n"
      "global_load_lds_dwordx4 %[u0], s[62:63]\n"
      "global_load_lds_dwordx4 %[u1], s[62:63] offset:1024\n"
      "global_load_lds_dwordx4 %[u2], s[62:63] offset:2048\n"
      "s_add_u32 s68, %[k0], 5\n"
      "s_cmp_ge_u32 s68, 10\n"
      "s_cselect_b32 s69, 10, 0\n"
      "s_sub_u32 s68, s68, s69\n"
      "s_mul_i32 s69, s68, 196608\n"
      "s_add_u32 s62, s74, s69\n"
      "s_addc_u32 s63, s75, 0\n"
      "s_add_u32 m0, %[l3], 114688\n"
      "s_waitcnt vmcnt(11)\n"
      "s_barrier\n"
      "global_load_lds_dwordx4 %[u0], s[62:63]\n"
      "global_load_lds_dwordx4 %[u1], s[62:63] offset:1024\n"
      "global_load_lds_dwordx4 %[u2], s[62:63] offset:2048\n"
      "s_add_u32 s68, %[k0], 6\n"
      "s_cmp_ge_u32 s68, 10\n"
      "s_cselect_b32 s69, 10, 0\n"
      "s_sub_u32 s68, s68, s69\n"
      "s_mul_i32 s69, s68, 196608\n"
      "s_add_u32 s62, s74, s69\n"
      "s_addc_u32 s63, s75, 0\n"
      "s_add_u32 m0, %[l3], 131072\n"
      "s_waitcnt vmcnt(6)\n"
      "s_barrier\n"
      "global_load_lds_dwordx4 %[u0], s[62:63]\n"
      "global_load_lds_dwordx4 %[u1], s[62:63] offset:1024\n"
      "global_load_lds_dwordx4 %[u2], s[62:63] offset:2048\n"
      "s_add_u32 s68, %[k0], 7\n"
      "s_cmp_ge_u32 s68, 10\n"
      "s_cselect_b32 s69, 10, 0\n"
      "s_sub_u32 s68, s68, s69\n"
      "s_mul_i32 s69, s68, 196608\n"
      "s_add_u32 s62, s74, s69\n"
      "s_addc_u32 s63, s75, 0\n"
      "s_add_u32 m0, %[l3], 98304\n"
      "s_waitcnt vmcnt(6)\n"
      "s_barrier\n"
      "global_load_lds_dwordx4 %[u0], s[62:63]\n"
      "global_load_lds_dwordx4 %[u1], s[62:63] offset:1024\n"
      "global_load_lds_dwordx4 %[u2], s[62:63] offset:2048\n"
      "s_add_u32 s68, %[k0], 8\n"
      "s_cmp_ge_u32 s68, 10\n"
      "s_cselect_b32 s69, 10, 0\n"
      "s_sub_u32 s68, s68, s69\n"
      "s_mul_i32 s69, s68, 196608\n"
      "s_add_u32 s62, s74, s69\n"
      "s_addc_u32 s63, s75, 0\n"
      "s_add_u32 m0, %[l3], 147456\n"
      "s_waitcnt vmcnt(3)\n"
      "s_barrier\n"
      "global_load_lds_dwordx4 %[u0], s[62:63]\n"
      "global_load_lds_dwordx4 %[u1], s[62:63] offset:1024\n"
      "global_load_lds_dwordx4 %[u2], s[62:63] offset:2048\n"
      "s_add_u32 s68, %[k0], 9\n"
      "s_cmp_ge_u32 s68, 10\n"
      "s_cselect_b32 s69, 10, 0\n"
      "s_sub_u32 s68, s68, s69\n"
      "s_mul_i32 s69, s68, 196608\n"
      "s_add_u32 s62, s74, s69\n"
      "s_addc_u32 s63, s75, 0\n"
      "s_add_u32 m0, %[l3], 114688\n"
      "s_waitcnt vmcnt(3)\n"
      "s_barrier\n"
      "global_load_lds_dwordx4 %[u0], s[62:63]\n"
      "global_load_lds_dwordx4 %[u1], s[62:63] offset:1024\n"
      "global_load_lds_dwordx4 %[u2], s[62:63] offset:2048\n"
      "s_add_u32 s68, %[k0], 3\n"
      "s_cmp_ge_u32 s68, 10\n"
      "s_cselect_b32 s69, 10, 0\n"
      "s_sub_u32 s68, s68, s69\n"
      "s_lshl_b32 s69, s68, 7\n"
      "s_add_u32 s60, s76, s69\n"
      "s_addc_u32 s61, s77, 0\n"
      "s_add_u32 m0, %[lw], 65536\n"
      "s_nop 0\n"
      "global_load_lds_dwordx4 %[x0], s[60:61]\n"
      "global_load_lds_dwordx4 %[x1], s[60:61] offset:1024\n"
      "global_load_lds_dwordx4 %[x2], s[60:61] offset:2048\n"
      "global_load_lds_dwordx4 %[x3], s[60:61] offset:3072\n"
      "s_add_u32 s68, %[k0], 3\n"
      "s_cmp_ge_u32 s68, 10\n"
      "s_cselect_b32 s69, 10, 0\n"
      "s_sub_u32 s68, s68, s69\n"
      "s_mul_i32 s69, s68, 196608\n"
      "s_add_u32 s62, s74, s69\n"
      "s_addc_u32 s63, s75, 0\n"
      "s_add_u32 m0, %[l3], 131072\n"
      "s_nop 0\n"
      "global_load_lds_dwordx4 %[u0], s[62:63]\n"
      "global_load_lds_dwordx4 %[u1], s[62:63] offset:1024\n"
      "global_load_lds_dwordx4 %[u2], s[62:63] offset:2048\n"
        :: [x0] "v"(x0), [x1] "v"(x1), [x2] "v"(x2), [x3] "v"(x3), [w0] "v"(w0), [w1] "v"(wo1), [w2] "v"(wo2), [w3] "v"(wo3),
           [u0] "v"(uo[0]), [u1] "v"(uo[1]), [u2] "v"(uo[2]),
           [xb] "s"(x + xoff), [wb] "s"(wqkv + woff), [ub] "s"(w1 + cb * 192),
           [k0] "s"(k0), [lw] "s"(lw), [l3] "s"(lbase + w * 3072)
        : "s60", "s61", "s62", "s63", "s64", "s65", "s66", "s67", "s68", "s69", "s70", "s71", "s72", "s73", "s74", "s75", "s76", "s77", "s78", "s79", "vcc", "scc", "memory");
      __builtin_amdgcn_s_setprio(3);
      u64 ep = kw + WS_FLAGS;
      asm volatile("" : "+s"(ep));
      const u64 tag = *(const CAS u64*)ep + 1;
      asm volatile("" :: "s"(tag));
      const bool two = (20 * head + 19) / 64 > (20 * head) / 64;
      const u32 l16 = ln * 16, fqq = (4 * qb + (ln & 3)) * 16;
      u64x2 fa, fb;
      auto fq_load = [&]() LAM {
        asm volatile("buffer_load_dwordx4 %0, %2, %4, %5 offen offset:512 sc1\n buffer_load_dwordx4 %1, %3, %4, %5 offen sc1"
                     : "=&v"(fa), "=&v"(fb) : "v"(l16), "v"(fqq), "s"(srd), "s"(WS_FQ + head * 2048) : "memory");
      };
      auto fq_ok = [&]() LAM {
        asm volatile("s_waitcnt vmcnt(0)" : "+v"(fa), "+v"(fb) :: "memory");
        return !__builtin_amdgcn_ballot_w64((fa.x != tag) | (fb.x != tag) | (two & ((fa.y != tag) | (fb.y != tag))));
      };
      vmwait<10>();
      bar();
      if (1 == 3) fq_load();
      LANEVARS
      const u32 kp0 = WS_KP + (4 + 2 * w) * 40960 + plA * 512 + l16, kp1 = kp0 + 40960 - 1024;
      const u32 kp2 = WS_KP + (4 + 2 * w + hh) * 40960 + plR * 512 + tok * 16 - 2048;
      auto vpiece = [&](u32 p) LAM {
        const u32 ix = 64 * p + lane, sg = (ix * 3277) >> 16;
        return WS_VT + (2 + 8 * w + sg + 2 * (sg & 2)) * 10240 + vcol(ix - 20 * sg) * 16 - 1024 * p;
      };
      const u32 vp0 = vpiece(0), vp1 = vpiece(1), vp2 = vpiece(2);
      asm volatile("" :: "v"(kp0), "v"(kp1), "v"(kp2), "v"(vp0), "v"(vp1), "v"(vp2));
      auto kv_issue = [&](u32 c, u32 slot) LAM {
        kv_k(kp0, kp1, kp2, ws + c * 327680, lbase + slot * KV_SLOT + 3072 * w);
        kv_v(vp0, vp1, vp2, ws + c * 327680, lbase + slot * KV_SLOT + KV_V + 2560 * w);
      };
      auto first_tiles = [&]() LAM {
        STAMP(8);
        kv_k(kp0, kp1, kp2, ws + 8 * qb * 40960 - (WS_KP + 163840), lbase + L_Q + QOFF(w));
        if (w < 2) kv_k(kp0, kp1, kp2, ws - 163840, lbase + L_PRE + 3072 * w);
        if (w == 3) kv_v(vp0, vp1, vp2, ws - 327680, lbase + L_PRE + 6144);
      };
      auto mark_o = [&](u32 lane) LAM {
        const u32x4 mark = {~0u, ~0u, ~0u, ~0u};
        const u32 ob = WS_O + (8 * qb + 4 * (w & 1)) * 40960 + (head >> 3) * 10240;
#pragma unroll
        for (int i = 0; i < 4; ++i) st128(mark, srd, lane * 16, ob + (head & 7) * 1024 + i * 40960);
        st128(mark, srd, (lane >> 4) * 40960 + (lane & 15) * 16, ob + 8192 + (head & 7) * 256);
      };
      vmwait<7 + (1 == 3 ? 2 : 0)>();
      bar();
      bool got = false;
      if (1 == 3) { got = fq_ok(); if (got) first_tiles(); } else vmwait<0>();
      if (1 == 2) fq_load();
      const u32 l2 = fresh_lane();
      const bool cvt = 0 ? !(xcd & 1) || (qc == 15 && w >= 2) : w < 2 || qc == 15;
      CVT_GEOMETRY(0 ? ((xcd & 1) ? 512 + 2 * rb + (w & 1) : 2 * b + w) : w < 2 ? b + 256 * w : 510 + 2 * rb + w)
      u32 co[5];
#pragma unroll
      for (u32 p = 0; p < 5; ++p) {
        const u32 ix = 64 * p + l2, r = (ix * 6554) >> 16;
        co[p] = (r + e8 * (r >> 3) + e16 * (r >> 4)) * 1280 + (ix - r * 10) * 16 - (p < 3 ? 1024 * p : 0);
      }
      const u32 cdst = isw2 ? WS_W2F + (ccg * 48 + cstep) * 5120 : WS_WOF + (ccg * 20 + os) * 5120;
      const u32 cra = L_CV + w * 5120 + (8 * (l2 >> 4) + q4) * 160;
      const u32 fhw = l2 < 8 ? WS_FH + l2 * 8192 + (4 * b + w) * 8 : WS_END, fhr = WS_FH + xcd * 8192 + w * 2048 + l16;
      const u32 hdst = WS_HP + (4 * rb + w) * 98304 + cb * 6144, sg = SY_G2 + 4 * p4;
      asm volatile("" :: "v"(co[0]), "v"(co[1]), "v"(co[2]), "v"(co[3]), "v"(co[4]), "s"(cvb), "s"(cdst), "v"(cra), "v"(fhw), "v"(fhr),
                   "v"(sg), "s"(hdst));
      bar();
      if (1 == 2) { got = fq_ok(); if (got) first_tiles(); }
      if (1 == 1) fq_load();
      bar();
      STAMP(2);
      if (1 == 1) { got = fq_ok(); if (got) first_tiles(); else __builtin_amdgcn_s_sleep(8); }
      if (!got) {
#pragma unroll 1
        for (u32 n = 0; n < SPIN_GLB; ++n) {
          fq_load();
          if (fq_ok()) break;
          __builtin_amdgcn_s_sleep(8);
        }
        first_tiles();
      }
      auto ready = [&](u32 c) LAM { dma_ready(ws + WS_FQ + head * 2048 + 512, lbase + SY_RD + 16 * c + 4 * w); };
      if (0) { vmwait<0>(); kv_issue(0, 0); } else { kv_issue(0, 0); vmwait<6>(); }
      bar();
      u32x4 hv[6];
#pragma unroll
      for (int i = 0; i < 6; ++i) hv[i] = *(LAS u32x4*)(L + L_H + w * 6144 + i * 1024 + l16);
      vmwait<0>();
      lds_post(L, SY_RD + 4 * w, (u32)tag);
      if (cvt) {
        kv_k(co[0], co[1], co[2], cvb, lbase + L_CV + w * 5120);
        dma1_sc1(co[3], cvb, lbase + L_CV + w * 5120 + 3072);
        dma1_sc1(co[4], cvb, lbase + L_CV + w * 5120 + 4096);
      }
      kv_issue(1, 2);
#pragma unroll
      for (int i = 0; i < 6; ++i) st128(hv[i], srd, l16, hdst + i * 1024);
      vmwait<6>();
      bf16x8 cf[5];
      if (cvt) {
#pragma unroll
        for (int t = 0; t < 5; ++t) cf[t] = trfrag(L + cra + (t < 4 ? 64 * (t >> 1) + 8 * (t & 1) + 16 * p4 : 128 + 8 * p4), 640);
      }
      lds_post(L, SY_RD + 16 + 4 * w, (u32)tag);
      if (cvt) {
#pragma unroll
        for (int t = 0; t < 5; ++t) st128(__builtin_bit_cast(u32x4, cf[t]), srd, l16, cdst + t * 1024);
      }
      if (w >= 2) mark_o(lane);
      kv_issue(2, 3);
      kv_issue(3, 4);
      vmwait<12>();
      __builtin_amdgcn_raw_buffer_store_b64(__builtin_bit_cast(u32x2, tag), rs, fhw, 0, SC1);
      STAMP(3);
      vmwait<7>();
      ready(2);
      lds_wait(L, SY_AD + (lane & 7) * 4, 2);
      kv_issue(4, 5);
      vmwait<6>();
      ready(3);
      kv_issue(5, 6);
      kv_issue(6, 1);
      vmwait<12>();
      ready(4);
      kv_issue(7, 0);
#pragma unroll 1
      for (u32 n = 0; n < SPIN_GLB; ++n) {
        bool miss = false;
#pragma unroll
        for (int i = 0; i < 2; ++i) {
          const u64x2 v = __builtin_bit_cast(u64x2, __builtin_amdgcn_raw_buffer_load_b128(rs, fhr + i * 1024, 0, SC1));
          miss |= (v.x != tag) | (v.y != tag);
        }
        vmwait<0>();
        if (n == 0) *(LAS u32*)(L + SY_RD + 16 * (5 + tmin(p4, 2u)) + 4 * w) = (u32)tag;
        if (!__builtin_amdgcn_ballot_w64(miss)) break;
        __builtin_amdgcn_s_sleep(16);
        asm volatile("" ::: "memory");
      }
      lds_post(L, SY_G2 + 4 * w, 2);
      lds_wait(L, sg, 2);
      if (b == 0 && w == 0 && lane == 0) __builtin_amdgcn_raw_buffer_store_b64(__builtin_bit_cast(u32x2, tag), rs, WS_FLAGS, 0, SC1);
      STAMP(4);
    }
    LANEVARS
    const u32 hbase = WS_HP + 2 * rt * 98304 + 12 * w * 2048, fo = (64 * rt + 16 * w) * 1280 + 160 * cg;
    asm volatile(
      "s_sub_u32 s60, %[rot], 1\n"
      "s_mov_b32 s66, 0x400000\n"
      "v_xor_b32 v167, 256, %[vh]\n"
      "s_add_u32 s60, s60, 1\n"
      "s_cmp_eq_u32 s60, 12\n"
      "s_cselect_b32 s60, 0, s60\n"
      "s_lshl_b32 s61, s60, 11\n"
      "s_add_u32 s65, s61, %[hb]\n"
      "s_add_u32 s61, s61, %[ha]\n"
      "s_mul_i32 s62, s60, 5120\n"
      "s_add_u32 s62, s62, %[wo]\n"
      "s_add_u32 s63, s62, 4096\n"
      "buffer_load_dwordx4 v[88:91], %[l16], %[rs], s62 offen offset:0 sc1\n"
      "buffer_load_dwordx4 v[92:95], %[l16], %[rs], s62 offen offset:1024 sc1\n"
      "buffer_load_dwordx4 v[96:99], %[l16], %[rs], s62 offen offset:2048 sc1\n"
      "buffer_load_dwordx4 v[100:103], %[l16], %[rs], s62 offen offset:3072 sc1\n"
      "buffer_load_dwordx4 v[104:107], %[l16], %[rs], s63 offen offset:0 sc1\n"
      "buffer_load_dwordx4 v[108:111], %[vh], %[rs], s61 offen offset:0 sc1\n"
      "buffer_load_dwordx4 v[112:115], v167, %[rs], s61 offen offset:0 sc1\n"
      "buffer_load_dwordx4 v[116:119], %[vh], %[rs], s65 offen offset:0 sc1\n"
      "buffer_load_dwordx4 v[120:123], v167, %[rs], s65 offen offset:0 sc1\n"
      "s_add_u32 s60, s60, 1\n"
      "s_cmp_eq_u32 s60, 12\n"
      "s_cselect_b32 s60, 0, s60\n"
      "s_lshl_b32 s61, s60, 11\n"
      "s_add_u32 s65, s61, %[hb]\n"
      "s_add_u32 s61, s61, %[ha]\n"
      "s_mul_i32 s62, s60, 5120\n"
      "s_add_u32 s62, s62, %[wo]\n"
      "s_add_u32 s63, s62, 4096\n"
      "buffer_load_dwordx4 v[124:127], %[l16], %[rs], s62 offen offset:0 sc1\n"
      "buffer_load_dwordx4 v[128:131], %[l16], %[rs], s62 offen offset:1024 sc1\n"
      "buffer_load_dwordx4 v[132:135], %[l16], %[rs], s62 offen offset:2048 sc1\n"
      "buffer_load_dwordx4 v[136:139], %[l16], %[rs], s62 offen offset:3072 sc1\n"
      "buffer_load_dwordx4 v[140:143], %[l16], %[rs], s63 offen offset:0 sc1\n"
      "buffer_load_dwordx4 v[144:147], %[vh], %[rs], s61 offen offset:0 sc1\n"
      "buffer_load_dwordx4 v[148:151], v167, %[rs], s61 offen offset:0 sc1\n"
      "buffer_load_dwordx4 v[152:155], %[vh], %[rs], s65 offen offset:0 sc1\n"
      "buffer_load_dwordx4 v[156:159], v167, %[rs], s65 offen offset:0 sc1\n"
      "s_setprio 3\n"
      "s_add_u32 s60, s60, 1\n"
      "s_cmp_eq_u32 s60, 12\n"
      "s_cselect_b32 s60, 0, s60\n"
      "s_lshl_b32 s61, s60, 11\n"
      "s_add_u32 s65, s61, %[hb]\n"
      "s_add_u32 s61, s61, %[ha]\n"
      "s_mul_i32 s62, s60, 5120\n"
      "s_add_u32 s62, s62, %[wo]\n"
      "s_add_u32 s63, s62, 4096\n"
      "s_waitcnt vmcnt(9)\n"
      "v_mfma_f32_16x16x32_bf16 v[8:11], v[88:91], v[108:111], 0\n"
      "s_nop 6\n"
      "v_mfma_f32_16x16x32_bf16 v[12:15], v[88:91], v[112:115], 0\n"
      "s_nop 6\n"
      "v_mfma_f32_16x16x32_bf16 v[16:19], v[88:91], v[116:119], 0\n"
      "s_nop 6\n"
      "v_mfma_f32_16x16x32_bf16 v[20:23], v[88:91], v[120:123], 0\n"
      "s_nop 6\n"
      "buffer_load_dwordx4 v[88:91], %[l16], %[rs], s62 offen offset:0 sc1\n"
      "v_mfma_f32_16x16x32_bf16 v[24:27], v[92:95], v[108:111], 0\n"
      "s_nop 6\n"
      "v_mfma_f32_16x16x32_bf16 v[28:31], v[92:95], v[112:115], 0\n"
      "s_nop 6\n"
      "v_mfma_f32_16x16x32_bf16 v[32:35], v[92:95], v[116:119], 0\n"
      "s_nop 6\n"
      "v_mfma_f32_16x16x32_bf16 v[36:39], v[92:95], v[120:123], 0\n"
      "s_nop 6\n"
      "buffer_load_dwordx4 v[92:95], %[l16], %[rs], s62 offen offset:1024 sc1\n"
      "v_mfma_f32_16x16x32_bf16 v[40:43], v[96:99], v[108:111], 0\n"
      "s_nop 6\n"
      "v_mfma_f32_16x16x32_bf16 v[44:47], v[96:99], v[112:115], 0\n"
      "s_nop 6\n"
      "v_mfma_f32_16x16x32_bf16 v[48:51], v[96:99], v[116:119], 0\n"
      "s_nop 6\n"
      "v_mfma_f32_16x16x32_bf16 v[52:55], v[96:99], v[120:123], 0\n"
      "s_nop 6\n"
      "buffer_load_dwordx4 v[96:99], %[l16], %[rs], s62 offen offset:2048 sc1\n"
      "v_mfma_f32_16x16x32_bf16 v[56:59], v[100:103], v[108:111], 0\n"
      "s_nop 6\n"
      "v_mfma_f32_16x16x32_bf16 v[60:63], v[100:103], v[112:115], 0\n"
      "s_nop 6\n"
      "v_mfma_f32_16x16x32_bf16 v[64:67], v[100:103], v[116:119], 0\n"
      "s_nop 6\n"
      "v_mfma_f32_16x16x32_bf16 v[68:71], v[100:103], v[120:123], 0\n"
      "s_nop 6\n"
      "buffer_load_dwordx4 v[100:103], %[l16], %[rs], s62 offen offset:3072 sc1\n"
      "v_mfma_f32_16x16x32_bf16 v[72:75], v[104:107], v[108:111], 0\n"
      "buffer_load_dwordx4 v[108:111], %[vh], %[rs], s61 offen offset:0 sc1\n"
      "s_nop 6\n"
      "v_mfma_f32_16x16x32_bf16 v[76:79], v[104:107], v[112:115], 0\n"
      "buffer_load_dwordx4 v[112:115], v167, %[rs], s61 offen offset:0 sc1\n"
      "s_nop 6\n"
      "v_mfma_f32_16x16x32_bf16 v[80:83], v[104:107], v[116:119], 0\n"
      "buffer_load_dwordx4 v[116:119], %[vh], %[rs], s65 offen offset:0 sc1\n"
      "s_nop 6\n"
      "v_mfma_f32_16x16x32_bf16 v[84:87], v[104:107], v[120:123], 0\n"
      "buffer_load_dwordx4 v[120:123], v167, %[rs], s65 offen offset:0 sc1\n"
      "s_nop 6\n"
      "buffer_load_dwordx4 v[104:107], %[l16], %[rs], s63 offen offset:0 sc1\n"
      "s_add_u32 s60, s60, 1\n"
      "s_cmp_eq_u32 s60, 12\n"
      "s_cselect_b32 s60, 0, s60\n"
      "s_lshl_b32 s61, s60, 11\n"
      "s_add_u32 s65, s61, %[hb]\n"
      "s_add_u32 s61, s61, %[ha]\n"
      "s_mul_i32 s62, s60, 5120\n"
      "s_add_u32 s62, s62, %[wo]\n"
      "s_add_u32 s63, s62, 4096\n"
      "s_waitcnt vmcnt(9)\n"
      "v_mfma_f32_16x16x32_bf16 v[8:11], v[124:127], v[144:147], v[8:11]\n"
      "s_nop 6\n"
      "v_mfma_f32_16x16x32_bf16 v[12:15], v[124:127], v[148:151], v[12:15]\n"
      "s_nop 6\n"
      "v_mfma_f32_16x16x32_bf16 v[16:19], v[124:127], v[152:155], v[16:19]\n"
      "s_nop 6\n"
      "v_mfma_f32_16x16x32_bf16 v[20:23], v[124:127], v[156:159], v[20:23]\n"
      "s_nop 6\n"
      "buffer_load_dwordx4 v[124:127], %[l16], %[rs], s62 offen offset:0 sc1\n"
      "v_mfma_f32_16x16x32_bf16 v[24:27], v[128:131], v[144:147], v[24:27]\n"
      "s_nop 6\n"
      "v_mfma_f32_16x16x32_bf16 v[28:31], v[128:131], v[148:151], v[28:31]\n"
      "s_nop 6\n"
      "v_mfma_f32_16x16x32_bf16 v[32:35], v[128:131], v[152:155], v[32:35]\n"
      "s_nop 6\n"
      "v_mfma_f32_16x16x32_bf16 v[36:39], v[128:131], v[156:159], v[36:39]\n"
      "s_nop 6\n"
      "buffer_load_dwordx4 v[128:131], %[l16], %[rs], s62 offen offset:1024 sc1\n"
      "v_mfma_f32_16x16x32_bf16 v[40:43], v[132:135], v[144:147], v[40:43]\n"
      "s_nop 6\n"
      "v_mfma_f32_16x16x32_bf16 v[44:47], v[132:135], v[148:151], v[44:47]\n"
      "s_nop 6\n"
      "v_mfma_f32_16x16x32_bf16 v[48:51], v[132:135], v[152:155], v[48:51]\n"
      "s_nop 6\n"
      "v_mfma_f32_16x16x32_bf16 v[52:55], v[132:135], v[156:159], v[52:55]\n"
      "s_nop 6\n"
      "buffer_load_dwordx4 v[132:135], %[l16], %[rs], s62 offen offset:2048 sc1\n"
      "v_mfma_f32_16x16x32_bf16 v[56:59], v[136:139], v[144:147], v[56:59]\n"
      "s_nop 6\n"
      "v_mfma_f32_16x16x32_bf16 v[60:63], v[136:139], v[148:151], v[60:63]\n"
      "s_nop 6\n"
      "v_mfma_f32_16x16x32_bf16 v[64:67], v[136:139], v[152:155], v[64:67]\n"
      "s_nop 6\n"
      "v_mfma_f32_16x16x32_bf16 v[68:71], v[136:139], v[156:159], v[68:71]\n"
      "s_nop 6\n"
      "buffer_load_dwordx4 v[136:139], %[l16], %[rs], s62 offen offset:3072 sc1\n"
      "v_mfma_f32_16x16x32_bf16 v[72:75], v[140:143], v[144:147], v[72:75]\n"
      "buffer_load_dwordx4 v[144:147], %[vh], %[rs], s61 offen offset:0 sc1\n"
      "s_nop 6\n"
      "v_mfma_f32_16x16x32_bf16 v[76:79], v[140:143], v[148:151], v[76:79]\n"
      "buffer_load_dwordx4 v[148:151], v167, %[rs], s61 offen offset:0 sc1\n"
      "s_nop 6\n"
      "v_mfma_f32_16x16x32_bf16 v[80:83], v[140:143], v[152:155], v[80:83]\n"
      "buffer_load_dwordx4 v[152:155], %[vh], %[rs], s65 offen offset:0 sc1\n"
      "s_nop 6\n"
      "v_mfma_f32_16x16x32_bf16 v[84:87], v[140:143], v[156:159], v[84:87]\n"
      "buffer_load_dwordx4 v[156:159], v167, %[rs], s65 offen offset:0 sc1\n"
      "s_nop 6\n"
      "buffer_load_dwordx4 v[140:143], %[l16], %[rs], s63 offen offset:0 sc1\n"
      "s_add_u32 s60, s60, 1\n"
      "s_cmp_eq_u32 s60, 12\n"
      "s_cselect_b32 s60, 0, s60\n"
      "s_lshl_b32 s61, s60, 11\n"
      "s_add_u32 s65, s61, %[hb]\n"
      "s_add_u32 s61, s61, %[ha]\n"
      "s_mul_i32 s62, s60, 5120\n"
      "s_add_u32 s62, s62, %[wo]\n"
      "s_add_u32 s63, s62, 4096\n"
      "s_waitcnt vmcnt(9)\n"
      "v_mfma_f32_16x16x32_bf16 v[8:11], v[88:91], v[108:111], v[8:11]\n"
      "s_nop 6\n"
      "v_mfma_f32_16x16x32_bf16 v[12:15], v[88:91], v[112:115], v[12:15]\n"
      "s_nop 6\n"
      "v_mfma_f32_16x16x32_bf16 v[16:19], v[88:91], v[116:119], v[16:19]\n"
      "s_nop 6\n"
      "v_mfma_f32_16x16x32_bf16 v[20:23], v[88:91], v[120:123], v[20:23]\n"
      "s_nop 6\n"
      "buffer_load_dwordx4 v[88:91], %[l16], %[rs], s62 offen offset:0 sc1\n"
      "v_mfma_f32_16x16x32_bf16 v[24:27], v[92:95], v[108:111], v[24:27]\n"
      "s_nop 6\n"
      "v_mfma_f32_16x16x32_bf16 v[28:31], v[92:95], v[112:115], v[28:31]\n"
      "s_nop 6\n"
      "v_mfma_f32_16x16x32_bf16 v[32:35], v[92:95], v[116:119], v[32:35]\n"
      "s_nop 6\n"
      "v_mfma_f32_16x16x32_bf16 v[36:39], v[92:95], v[120:123], v[36:39]\n"
      "s_nop 6\n"
      "buffer_load_dwordx4 v[92:95], %[l16], %[rs], s62 offen offset:1024 sc1\n"
      "v_mfma_f32_16x16x32_bf16 v[40:43], v[96:99], v[108:111], v[40:43]\n"
      "s_nop 6\n"
      "v_mfma_f32_16x16x32_bf16 v[44:47], v[96:99], v[112:115], v[44:47]\n"
      "s_nop 6\n"
      "v_mfma_f32_16x16x32_bf16 v[48:51], v[96:99], v[116:119], v[48:51]\n"
      "s_nop 6\n"
      "v_mfma_f32_16x16x32_bf16 v[52:55], v[96:99], v[120:123], v[52:55]\n"
      "s_nop 6\n"
      "buffer_load_dwordx4 v[96:99], %[l16], %[rs], s62 offen offset:2048 sc1\n"
      "v_mfma_f32_16x16x32_bf16 v[56:59], v[100:103], v[108:111], v[56:59]\n"
      "s_nop 6\n"
      "v_mfma_f32_16x16x32_bf16 v[60:63], v[100:103], v[112:115], v[60:63]\n"
      "s_nop 6\n"
      "v_mfma_f32_16x16x32_bf16 v[64:67], v[100:103], v[116:119], v[64:67]\n"
      "s_nop 6\n"
      "v_mfma_f32_16x16x32_bf16 v[68:71], v[100:103], v[120:123], v[68:71]\n"
      "s_nop 6\n"
      "buffer_load_dwordx4 v[100:103], %[l16], %[rs], s62 offen offset:3072 sc1\n"
      "v_mfma_f32_16x16x32_bf16 v[72:75], v[104:107], v[108:111], v[72:75]\n"
      "buffer_load_dwordx4 v[108:111], %[vh], %[rs], s61 offen offset:0 sc1\n"
      "s_nop 6\n"
      "v_mfma_f32_16x16x32_bf16 v[76:79], v[104:107], v[112:115], v[76:79]\n"
      "buffer_load_dwordx4 v[112:115], v167, %[rs], s61 offen offset:0 sc1\n"
      "s_nop 6\n"
      "v_mfma_f32_16x16x32_bf16 v[80:83], v[104:107], v[116:119], v[80:83]\n"
      "buffer_load_dwordx4 v[116:119], %[vh], %[rs], s65 offen offset:0 sc1\n"
      "s_nop 6\n"
      "v_mfma_f32_16x16x32_bf16 v[84:87], v[104:107], v[120:123], v[84:87]\n"
      "buffer_load_dwordx4 v[120:123], v167, %[rs], s65 offen offset:0 sc1\n"
      "s_nop 6\n"
      "buffer_load_dwordx4 v[104:107], %[l16], %[rs], s63 offen offset:0 sc1\n"
      "s_add_u32 s60, s60, 1\n"
      "s_cmp_eq_u32 s60, 12\n"
      "s_cselect_b32 s60, 0, s60\n"
      "s_lshl_b32 s61, s60, 11\n"
      "s_add_u32 s65, s61, %[hb]\n"
      "s_add_u32 s61, s61, %[ha]\n"
      "s_mul_i32 s62, s60, 5120\n"
      "s_add_u32 s62, s62, %[wo]\n"
      "s_add_u32 s63, s62, 4096\n"
      "s_waitcnt vmcnt(9)\n"
      "v_mfma_f32_16x16x32_bf16 v[8:11], v[124:127], v[144:147], v[8:11]\n"
      "s_nop 6\n"
      "v_mfma_f32_16x16x32_bf16 v[12:15], v[124:127], v[148:151], v[12:15]\n"
      "s_nop 6\n"
      "v_mfma_f32_16x16x32_bf16 v[16:19], v[124:127], v[152:155], v[16:19]\n"
      "s_nop 6\n"
      "v_mfma_f32_16x16x32_bf16 v[20:23], v[124:127], v[156:159], v[20:23]\n"
      "s_nop 6\n"
      "buffer_load_dwordx4 v[124:127], %[l16], %[rs], s62 offen offset:0 sc1\n"
      "v_mfma_f32_16x16x32_bf16 v[24:27], v[128:131], v[144:147], v[24:27]\n"
      "s_nop 6\n"
      "v_mfma_f32_16x16x32_bf16 v[28:31], v[128:131], v[148:151], v[28:31]\n"
      "s_nop 6\n"
      "v_mfma_f32_16x16x32_bf16 v[32:35], v[128:131], v[152:155], v[32:35]\n"
      "s_nop 6\n"
      "v_mfma_f32_16x16x32_bf16 v[36:39], v[128:131], v[156:159], v[36:39]\n"
      "s_nop 6\n"
      "buffer_load_dwordx4 v[128:131], %[l16], %[rs], s62 offen offset:1024 sc1\n"
      "v_mfma_f32_16x16x32_bf16 v[40:43], v[132:135], v[144:147], v[40:43]\n"
      "s_nop 6\n"
      "v_mfma_f32_16x16x32_bf16 v[44:47], v[132:135], v[148:151], v[44:47]\n"
      "s_nop 6\n"
      "v_mfma_f32_16x16x32_bf16 v[48:51], v[132:135], v[152:155], v[48:51]\n"
      "s_nop 6\n"
      "v_mfma_f32_16x16x32_bf16 v[52:55], v[132:135], v[156:159], v[52:55]\n"
      "s_nop 6\n"
      "buffer_load_dwordx4 v[132:135], %[l16], %[rs], s62 offen offset:2048 sc1\n"
      "v_mfma_f32_16x16x32_bf16 v[56:59], v[136:139], v[144:147], v[56:59]\n"
      "s_nop 6\n"
      "v_mfma_f32_16x16x32_bf16 v[60:63], v[136:139], v[148:151], v[60:63]\n"
      "s_nop 6\n"
      "v_mfma_f32_16x16x32_bf16 v[64:67], v[136:139], v[152:155], v[64:67]\n"
      "s_nop 6\n"
      "v_mfma_f32_16x16x32_bf16 v[68:71], v[136:139], v[156:159], v[68:71]\n"
      "s_nop 6\n"
      "buffer_load_dwordx4 v[136:139], %[l16], %[rs], s62 offen offset:3072 sc1\n"
      "v_mfma_f32_16x16x32_bf16 v[72:75], v[140:143], v[144:147], v[72:75]\n"
      "buffer_load_dwordx4 v[144:147], %[vh], %[rs], s61 offen offset:0 sc1\n"
      "s_nop 6\n"
      "v_mfma_f32_16x16x32_bf16 v[76:79], v[140:143], v[148:151], v[76:79]\n"
      "buffer_load_dwordx4 v[148:151], v167, %[rs], s61 offen offset:0 sc1\n"
      "s_nop 6\n"
      "v_mfma_f32_16x16x32_bf16 v[80:83], v[140:143], v[152:155], v[80:83]\n"
      "buffer_load_dwordx4 v[152:155], %[vh], %[rs], s65 offen offset:0 sc1\n"
      "s_nop 6\n"
      "v_mfma_f32_16x16x32_bf16 v[84:87], v[140:143], v[156:159], v[84:87]\n"
      "buffer_load_dwordx4 v[156:159], v167, %[rs], s65 offen offset:0 sc1\n"
      "s_nop 6\n"
      "buffer_load_dwordx4 v[140:143], %[l16], %[rs], s63 offen offset:0 sc1\n"
      "s_add_u32 s60, s60, 1\n"
      "s_cmp_eq_u32 s60, 12\n"
      "s_cselect_b32 s60, 0, s60\n"
      "s_lshl_b32 s61, s60, 11\n"
      "s_add_u32 s65, s61, %[hb]\n"
      "s_add_u32 s61, s61, %[ha]\n"
      "s_mul_i32 s62, s60, 5120\n"
      "s_add_u32 s62, s62, %[wo]\n"
      "s_add_u32 s63, s62, 4096\n"
      "s_waitcnt vmcnt(9)\n"
      "v_mfma_f32_16x16x32_bf16 v[8:11], v[88:91], v[108:111], v[8:11]\n"
      "s_nop 6\n"
      "v_mfma_f32_16x16x32_bf16 v[12:15], v[88:91], v[112:115], v[12:15]\n"
      "s_nop 6\n"
      "v_mfma_f32_16x16x32_bf16 v[16:19], v[88:91], v[116:119], v[16:19]\n"
      "s_nop 6\n"
      "v_mfma_f32_16x16x32_bf16 v[20:23], v[88:91], v[120:123], v[20:23]\n"
      "s_nop 6\n"
      "buffer_load_dwordx4 v[88:91], %[l16], %[rs], s62 offen offset:0 sc1\n"
      "v_mfma_f32_16x16x32_bf16 v[24:27], v[92:95], v[108:111], v[24:27]\n"
      "s_nop 6\n"
      "v_mfma_f32_16x16x32_bf16 v[28:31], v[92:95], v[112:115], v[28:31]\n"
      "s_nop 6\n"
      "v_mfma_f32_16x16x32_bf16 v[32:35], v[92:95], v[116:119], v[32:35]\n"
      "s_nop 6\n"
      "v_mfma_f32_16x16x32_bf16 v[36:39], v[92:95], v[120:123], v[36:39]\n"
      "s_nop 6\n"
      "buffer_load_dwordx4 v[92:95], %[l16], %[rs], s62 offen offset:1024 sc1\n"
      "v_mfma_f32_16x16x32_bf16 v[40:43], v[96:99], v[108:111], v[40:43]\n"
      "s_nop 6\n"
      "v_mfma_f32_16x16x32_bf16 v[44:47], v[96:99], v[112:115], v[44:47]\n"
      "s_nop 6\n"
      "v_mfma_f32_16x16x32_bf16 v[48:51], v[96:99], v[116:119], v[48:51]\n"
      "s_nop 6\n"
      "v_mfma_f32_16x16x32_bf16 v[52:55], v[96:99], v[120:123], v[52:55]\n"
      "s_nop 6\n"
      "buffer_load_dwordx4 v[96:99], %[l16], %[rs], s62 offen offset:2048 sc1\n"
      "v_mfma_f32_16x16x32_bf16 v[56:59], v[100:103], v[108:111], v[56:59]\n"
      "s_nop 6\n"
      "v_mfma_f32_16x16x32_bf16 v[60:63], v[100:103], v[112:115], v[60:63]\n"
      "s_nop 6\n"
      "v_mfma_f32_16x16x32_bf16 v[64:67], v[100:103], v[116:119], v[64:67]\n"
      "s_nop 6\n"
      "v_mfma_f32_16x16x32_bf16 v[68:71], v[100:103], v[120:123], v[68:71]\n"
      "s_nop 6\n"
      "buffer_load_dwordx4 v[100:103], %[l16], %[rs], s62 offen offset:3072 sc1\n"
      "v_mfma_f32_16x16x32_bf16 v[72:75], v[104:107], v[108:111], v[72:75]\n"
      "buffer_load_dwordx4 v[108:111], %[vh], %[rs], s61 offen offset:0 sc1\n"
      "s_nop 6\n"
      "v_mfma_f32_16x16x32_bf16 v[76:79], v[104:107], v[112:115], v[76:79]\n"
      "buffer_load_dwordx4 v[112:115], v167, %[rs], s61 offen offset:0 sc1\n"
      "s_nop 6\n"
      "v_mfma_f32_16x16x32_bf16 v[80:83], v[104:107], v[116:119], v[80:83]\n"
      "buffer_load_dwordx4 v[116:119], %[vh], %[rs], s65 offen offset:0 sc1\n"
      "s_nop 6\n"
      "v_mfma_f32_16x16x32_bf16 v[84:87], v[104:107], v[120:123], v[84:87]\n"
      "buffer_load_dwordx4 v[120:123], v167, %[rs], s65 offen offset:0 sc1\n"
      "s_nop 6\n"
      "buffer_load_dwordx4 v[104:107], %[l16], %[rs], s63 offen offset:0 sc1\n"
      "s_setprio 3\n"
      "Lwa5_%=:\n"
      "ds_read_b32 v160, %[ad]\n"
      "s_waitcnt lgkmcnt(0)\n"
      "v_cmp_gt_u32 vcc, 5, v160\n"
      "s_cbranch_vccz Lga5_%=\n"
      "s_sleep 2\n"
      "s_sub_u32 s66, s66, 1\n"
      "s_cbranch_scc0 Lwa5_%=\n"
      "s_mov_b32 s66, 0\n"
      "Lga5_%=:\n"
      "s_setprio 3\n"
      "s_add_u32 s60, s60, 1\n"
      "s_cmp_eq_u32 s60, 12\n"
      "s_cselect_b32 s60, 0, s60\n"
      "s_lshl_b32 s61, s60, 11\n"
      "s_add_u32 s65, s61, %[hb]\n"
      "s_add_u32 s61, s61, %[ha]\n"
      "s_mul_i32 s62, s60, 5120\n"
      "s_add_u32 s62, s62, %[wo]\n"
      "s_add_u32 s63, s62, 4096\n"
      "s_waitcnt vmcnt(9)\n"
      "s_add_u32 m0, %[lw], 0\n"
      "s_add_u32 s64, %[fw], 0\n"
      "buffer_load_dwordx4 %[l16], %[rs], s64 offen sc1 lds\n"
      "v_mfma_f32_16x16x32_bf16 v[8:11], v[124:127], v[144:147], v[8:11]\n"
      "s_nop 6\n"
      "v_mfma_f32_16x16x32_bf16 v[12:15], v[124:127], v[148:151], v[12:15]\n"
      "s_nop 6\n"
      "v_mfma_f32_16x16x32_bf16 v[16:19], v[124:127], v[152:155], v[16:19]\n"
      "s_nop 6\n"
      "v_mfma_f32_16x16x32_bf16 v[20:23], v[124:127], v[156:159], v[20:23]\n"
      "s_nop 6\n"
      "buffer_load_dwordx4 v[124:127], %[l16], %[rs], s62 offen offset:0 sc1\n"
      "s_add_u32 m0, %[lw], 4096\n"
      "s_add_u32 s64, %[fw], 4096\n"
      "buffer_load_dwordx4 %[l16], %[rs], s64 offen sc1 lds\n"
      "v_mfma_f32_16x16x32_bf16 v[24:27], v[128:131], v[144:147], v[24:27]\n"
      "s_nop 6\n"
      "v_mfma_f32_16x16x32_bf16 v[28:31], v[128:131], v[148:151], v[28:31]\n"
      "s_nop 6\n"
      "v_mfma_f32_16x16x32_bf16 v[32:35], v[128:131], v[152:155], v[32:35]\n"
      "s_nop 6\n"
      "v_mfma_f32_16x16x32_bf16 v[36:39], v[128:131], v[156:159], v[36:39]\n"
      "s_nop 6\n"
      "buffer_load_dwordx4 v[128:131], %[l16], %[rs], s62 offen offset:1024 sc1\n"
      "s_add_u32 m0, %[lw], 8192\n"
      "s_add_u32 s64, %[fw], 8192\n"
      "buffer_load_dwordx4 %[l16], %[rs], s64 offen sc1 lds\n"
      "v_mfma_f32_16x16x32_bf16 v[40:43], v[132:135], v[144:147], v[40:43]\n"
      "s_nop 6\n"
      "v_mfma_f32_16x16x32_bf16 v[44:47], v[132:135], v[148:151], v[44:47]\n"
      "s_nop 6\n"
      "v_mfma_f32_16x16x32_bf16 v[48:51], v[132:135], v[152:155], v[48:51]\n"
      "s_nop 6\n"
      "v_mfma_f32_16x16x32_bf16 v[52:55], v[132:135], v[156:159], v[52:55]\n"
      "s_nop 6\n"
      "buffer_load_dwordx4 v[132:135], %[l16], %[rs], s62 offen offset:2048 sc1\n"
      "v_mfma_f32_16x16x32_bf16 v[56:59], v[136:139], v[144:147], v[56:59]\n"
      "s_nop 6\n"
      "v_mfma_f32_16x16x32_bf16 v[60:63], v[136:139], v[148:151], v[60:63]\n"
      "s_nop 6\n"
      "v_mfma_f32_16x16x32_bf16 v[64:67], v[136:139], v[152:155], v[64:67]\n"
      "s_nop 6\n"
      "v_mfma_f32_16x16x32_bf16 v[68:71], v[136:139], v[156:159], v[68:71]\n"
      "s_nop 6\n"
      "buffer_load_dwordx4 v[136:139], %[l16], %[rs], s62 offen offset:3072 sc1\n"
      "v_mfma_f32_16x16x32_bf16 v[72:75], v[140:143], v[144:147], v[72:75]\n"
      "buffer_load_dwordx4 v[144:147], %[vh], %[rs], s61 offen offset:0 sc1\n"
      "s_nop 6\n"
      "v_mfma_f32_16x16x32_bf16 v[76:79], v[140:143], v[148:151], v[76:79]\n"
      "buffer_load_dwordx4 v[148:151], v167, %[rs], s61 offen offset:0 sc1\n"
      "s_nop 6\n"
      "v_mfma_f32_16x16x32_bf16 v[80:83], v[140:143], v[152:155], v[80:83]\n"
      "buffer_load_dwordx4 v[152:155], %[vh], %[rs], s65 offen offset:0 sc1\n"
      "s_nop 6\n"
      "v_mfma_f32_16x16x32_bf16 v[84:87], v[140:143], v[156:159], v[84:87]\n"
      "buffer_load_dwordx4 v[156:159], v167, %[rs], s65 offen offset:0 sc1\n"
      "s_nop 6\n"
      "buffer_load_dwordx4 v[140:143], %[l16], %[rs], s63 offen offset:0 sc1\n"
      "s_add_u32 s60, s60, 1\n"
      "s_cmp_eq_u32 s60, 12\n"
      "s_cselect_b32 s60, 0, s60\n"
      "s_lshl_b32 s61, s60, 11\n"
      "s_add_u32 s65, s61, %[hb]\n"
      "s_add_u32 s61, s61, %[ha]\n"
      "s_mul_i32 s62, s60, 5120\n"
      "s_add_u32 s62, s62, %[wo]\n"
      "s_add_u32 s63, s62, 4096\n"
      "s_waitcnt vmcnt(12)\n"
      "s_add_u32 m0, %[lw], 12288\n"
      "s_add_u32 s64, %[fw], 12288\n"
      "buffer_load_dwordx4 %[l16], %[rs], s64 offen sc1 lds\n"
      "v_mfma_f32_16x16x32_bf16 v[8:11], v[88:91], v[108:111], v[8:11]\n"
      "s_nop 6\n"
      "v_mfma_f32_16x16x32_bf16 v[12:15], v[88:91], v[112:115], v[12:15]\n"
      "s_nop 6\n"
      "v_mfma_f32_16x16x32_bf16 v[16:19], v[88:91], v[116:119], v[16:19]\n"
      "s_nop 6\n"
      "v_mfma_f32_16x16x32_bf16 v[20:23], v[88:91], v[120:123], v[20:23]\n"
      "s_nop 6\n"
      "buffer_load_dwordx4 v[88:91], %[l16], %[rs], s62 offen offset:0 sc1\n"
      "s_add_u32 m0, %[lw], 16384\n"
      "s_add_u32 s64, %[fw], 16384\n"
      "buffer_load_dwordx4 %[l16], %[rs], s64 offen sc1 lds\n"
      "v_mfma_f32_16x16x32_bf16 v[24:27], v[92:95], v[108:111], v[24:27]\n"
      "s_nop 6\n"
      "v_mfma_f32_16x16x32_bf16 v[28:31], v[92:95], v[112:115], v[28:31]\n"
      "s_nop 6\n"
      "v_mfma_f32_16x16x32_bf16 v[32:35], v[92:95], v[116:119], v[32:35]\n"
      "s_nop 6\n"
      "v_mfma_f32_16x16x32_bf16 v[36:39], v[92:95], v[120:123], v[36:39]\n"
      "s_nop 6\n"
      "buffer_load_dwordx4 v[92:95], %[l16], %[rs], s62 offen offset:1024 sc1\n"
      "s_add_u32 m0, %[lw], 20480\n"
      "s_add_u32 s64, %[fw], 20480\n"
      "buffer_load_dwordx4 %[l16], %[rs], s64 offen sc1 lds\n"
      "v_mfma_f32_16x16x32_bf16 v[40:43], v[96:99], v[108:111], v[40:43]\n"
      "s_nop 6\n"
      "v_mfma_f32_16x16x32_bf16 v[44:47], v[96:99], v[112:115], v[44:47]\n"
      "s_nop 6\n"
      "v_mfma_f32_16x16x32_bf16 v[48:51], v[96:99], v[116:119], v[48:51]\n"
      "s_nop 6\n"
      "v_mfma_f32_16x16x32_bf16 v[52:55], v[96:99], v[120:123], v[52:55]\n"
      "s_nop 6\n"
      "buffer_load_dwordx4 v[96:99], %[l16], %[rs], s62 offen offset:2048 sc1\n"
      "v_mfma_f32_16x16x32_bf16 v[56:59], v[100:103], v[108:111], v[56:59]\n"
      "s_nop 6\n"
      "v_mfma_f32_16x16x32_bf16 v[60:63], v[100:103], v[112:115], v[60:63]\n"
      "s_nop 6\n"
      "v_mfma_f32_16x16x32_bf16 v[64:67], v[100:103], v[116:119], v[64:67]\n"
      "s_nop 6\n"
      "v_mfma_f32_16x16x32_bf16 v[68:71], v[100:103], v[120:123], v[68:71]\n"
      "s_nop 6\n"
      "buffer_load_dwordx4 v[100:103], %[l16], %[rs], s62 offen offset:3072 sc1\n"
      "v_mfma_f32_16x16x32_bf16 v[72:75], v[104:107], v[108:111], v[72:75]\n"
      "buffer_load_dwordx4 v[108:111], %[vh], %[rs], s61 offen offset:0 sc1\n"
      "s_nop 6\n"
      "v_mfma_f32_16x16x32_bf16 v[76:79], v[104:107], v[112:115], v[76:79]\n"
      "buffer_load_dwordx4 v[112:115], v167, %[rs], s61 offen offset:0 sc1\n"
      "s_nop 6\n"
      "v_mfma_f32_16x16x32_bf16 v[80:83], v[104:107], v[116:119], v[80:83]\n"
      "buffer_load_dwordx4 v[116:119], %[vh], %[rs], s65 offen offset:0 sc1\n"
      "s_nop 6\n"
      "v_mfma_f32_16x16x32_bf16 v[84:87], v[104:107], v[120:123], v[84:87]\n"
      "buffer_load_dwordx4 v[120:123], v167, %[rs], s65 offen offset:0 sc1\n"
      "s_nop 6\n"
      "buffer_load_dwordx4 v[104:107], %[l16], %[rs], s63 offen offset:0 sc1\n"
      "s_add_u32 s60, s60, 1\n"
      "s_cmp_eq_u32 s60, 12\n"
      "s_cselect_b32 s60, 0, s60\n"
      "s_lshl_b32 s61, s60, 11\n"
      "s_add_u32 s65, s61, %[hb]\n"
      "s_add_u32 s61, s61, %[ha]\n"
      "s_mul_i32 s62, s60, 5120\n"
      "s_add_u32 s62, s62, %[wo]\n"
      "s_add_u32 s63, s62, 4096\n"
      "s_waitcnt vmcnt(12)\n"
      "s_add_u32 m0, %[lw], 24576\n"
      "s_add_u32 s64, %[fw], 24576\n"
      "buffer_load_dwordx4 %[l16], %[rs], s64 offen sc1 lds\n"
      "v_mfma_f32_16x16x32_bf16 v[8:11], v[124:127], v[144:147], v[8:11]\n"
      "s_nop 6\n"
      "v_mfma_f32_16x16x32_bf16 v[12:15], v[124:127], v[148:151], v[12:15]\n"
      "s_nop 6\n"
      "v_mfma_f32_16x16x32_bf16 v[16:19], v[124:127], v[152:155], v[16:19]\n"
      "s_nop 6\n"
      "v_mfma_f32_16x16x32_bf16 v[20:23], v[124:127], v[156:159], v[20:23]\n"
      "s_nop 6\n"
      "buffer_load_dwordx4 v[124:127], %[l16], %[rs], s62 offen offset:0 sc1\n"
      "s_add_u32 m0, %[lw], 28672\n"
      "s_add_u32 s64, %[fw], 28672\n"
      "buffer_load_dwordx4 %[l16], %[rs], s64 offen sc1 lds\n"
      "v_mfma_f32_16x16x32_bf16 v[24:27], v[128:131], v[144:147], v[24:27]\n"
      "s_nop 6\n"
      "v_mfma_f32_16x16x32_bf16 v[28:31], v[128:131], v[148:151], v[28:31]\n"
      "s_nop 6\n"
      "v_mfma_f32_16x16x32_bf16 v[32:35], v[128:131], v[152:155], v[32:35]\n"
      "s_nop 6\n"
      "v_mfma_f32_16x16x32_bf16 v[36:39], v[128:131], v[156:159], v[36:39]\n"
      "s_nop 6\n"
      "buffer_load_dwordx4 v[128:131], %[l16], %[rs], s62 offen offset:1024 sc1\n"
      "s_add_u32 m0, %[lw], 32768\n"
      "s_add_u32 s64, %[fw], 32768\n"
      "buffer_load_dwordx4 %[l16], %[rs], s64 offen sc1 lds\n"
      "v_mfma_f32_16x16x32_bf16 v[40:43], v[132:135], v[144:147], v[40:43]\n"
      "s_nop 6\n"
      "v_mfma_f32_16x16x32_bf16 v[44:47], v[132:135], v[148:151], v[44:47]\n"
      "s_nop 6\n"
      "v_mfma_f32_16x16x32_bf16 v[48:51], v[132:135], v[152:155], v[48:51]\n"
      "s_nop 6\n"
      "v_mfma_f32_16x16x32_bf16 v[52:55], v[132:135], v[156:159], v[52:55]\n"
      "s_nop 6\n"
      "buffer_load_dwordx4 v[132:135], %[l16], %[rs], s62 offen offset:2048 sc1\n"
      "v_mfma_f32_16x16x32_bf16 v[56:59], v[136:139], v[144:147], v[56:59]\n"
      "s_nop 6\n"
      "v_mfma_f32_16x16x32_bf16 v[60:63], v[136:139], v[148:151], v[60:63]\n"
      "s_nop 6\n"
      "v_mfma_f32_16x16x32_bf16 v[64:67], v[136:139], v[152:155], v[64:67]\n"
      "s_nop 6\n"
      "v_mfma_f32_16x16x32_bf16 v[68:71], v[136:139], v[156:159], v[68:71]\n"
      "s_nop 6\n"
      "buffer_load_dwordx4 v[136:139], %[l16], %[rs], s62 offen offset:3072 sc1\n"
      "v_mfma_f32_16x16x32_bf16 v[72:75], v[140:143], v[144:147], v[72:75]\n"
      "buffer_load_dwordx4 v[144:147], %[vh], %[rs], s61 offen offset:0 sc1\n"
      "s_nop 6\n"
      "v_mfma_f32_16x16x32_bf16 v[76:79], v[140:143], v[148:151], v[76:79]\n"
      "buffer_load_dwordx4 v[148:151], v167, %[rs], s61 offen offset:0 sc1\n"
      "s_nop 6\n"
      "v_mfma_f32_16x16x32_bf16 v[80:83], v[140:143], v[152:155], v[80:83]\n"
      "buffer_load_dwordx4 v[152:155], %[vh], %[rs], s65 offen offset:0 sc1\n"
      "s_nop 6\n"
      "v_mfma_f32_16x16x32_bf16 v[84:87], v[140:143], v[156:159], v[84:87]\n"
      "buffer_load_dwordx4 v[156:159], v167, %[rs], s65 offen offset:0 sc1\n"
      "s_nop 6\n"
      "buffer_load_dwordx4 v[140:143], %[l16], %[rs], s63 offen offset:0 sc1\n"
      "s_add_u32 s60, s60, 1\n"
      "s_cmp_eq_u32 s60, 12\n"
      "s_cselect_b32 s60, 0, s60\n"
      "s_lshl_b32 s61, s60, 11\n"
      "s_add_u32 s65, s61, %[hb]\n"
      "s_add_u32 s61, s61, %[ha]\n"
      "s_mul_i32 s62, s60, 5120\n"
      "s_add_u32 s62, s62, %[wo]\n"
      "s_add_u32 s63, s62, 4096\n"
      "s_waitcnt vmcnt(12)\n"
      "s_add_u32 m0, %[lw], 36864\n"
      "s_add_u32 s64, %[fw], 36864\n"
      "buffer_load_dwordx4 %[l16], %[rs], s64 offen sc1 lds\n"
      "v_mfma_f32_16x16x32_bf16 v[8:11], v[88:91], v[108:111], v[8:11]\n"
      "s_nop 6\n"
      "v_mfma_f32_16x16x32_bf16 v[12:15], v[88:91], v[112:115], v[12:15]\n"
      "s_nop 6\n"
      "v_mfma_f32_16x16x32_bf16 v[16:19], v[88:91], v[116:119], v[16:19]\n"
      "s_nop 6\n"
      "v_mfma_f32_16x16x32_bf16 v[20:23], v[88:91], v[120:123], v[20:23]\n"
      "s_nop 6\n"
      "buffer_load_dwordx4 v[88:91], %[l16], %[rs], s62 offen offset:0 sc1\n"
      "s_add_u32 m0, %[lw], 40960\n"
      "s_add_u32 s64, %[fw], 40960\n"
      "buffer_load_dwordx4 %[l16], %[rs], s64 offen sc1 lds\n"
      "v_mfma_f32_16x16x32_bf16 v[24:27], v[92:95], v[108:111], v[24:27]\n"
      "s_nop 6\n"
      "v_mfma_f32_16x16x32_bf16 v[28:31], v[92:95], v[112:115], v[28:31]\n"
      "s_nop 6\n"
      "v_mfma_f32_16x16x32_bf16 v[32:35], v[92:95], v[116:119], v[32:35]\n"
      "s_nop 6\n"
      "v_mfma_f32_16x16x32_bf16 v[36:39], v[92:95], v[120:123], v[36:39]\n"
      "s_nop 6\n"
      "buffer_load_dwordx4 v[92:95], %[l16], %[rs], s62 offen offset:1024 sc1\n"
      "s_add_u32 m0, %[lw], 45056\n"
      "s_add_u32 s64, %[fw], 45056\n"
      "buffer_load_dwordx4 %[l16], %[rs], s64 offen sc1 lds\n"
      "v_mfma_f32_16x16x32_bf16 v[40:43], v[96:99], v[108:111], v[40:43]\n"
      "s_nop 6\n"
      "v_mfma_f32_16x16x32_bf16 v[44:47], v[96:99], v[112:115], v[44:47]\n"
      "s_nop 6\n"
      "v_mfma_f32_16x16x32_bf16 v[48:51], v[96:99], v[116:119], v[48:51]\n"
      "s_nop 6\n"
      "v_mfma_f32_16x16x32_bf16 v[52:55], v[96:99], v[120:123], v[52:55]\n"
      "s_nop 6\n"
      "buffer_load_dwordx4 v[96:99], %[l16], %[rs], s62 offen offset:2048 sc1\n"
      "v_mfma_f32_16x16x32_bf16 v[56:59], v[100:103], v[108:111], v[56:59]\n"
      "s_nop 6\n"
      "v_mfma_f32_16x16x32_bf16 v[60:63], v[100:103], v[112:115], v[60:63]\n"
      "s_nop 6\n"
      "v_mfma_f32_16x16x32_bf16 v[64:67], v[100:103], v[116:119], v[64:67]\n"
      "s_nop 6\n"
      "v_mfma_f32_16x16x32_bf16 v[68:71], v[100:103], v[120:123], v[68:71]\n"
      "s_nop 6\n"
      "buffer_load_dwordx4 v[100:103], %[l16], %[rs], s62 offen offset:3072 sc1\n"
      "v_mfma_f32_16x16x32_bf16 v[72:75], v[104:107], v[108:111], v[72:75]\n"
      "buffer_load_dwordx4 v[108:111], %[vh], %[rs], s61 offen offset:0 sc1\n"
      "s_nop 6\n"
      "v_mfma_f32_16x16x32_bf16 v[76:79], v[104:107], v[112:115], v[76:79]\n"
      "buffer_load_dwordx4 v[112:115], v167, %[rs], s61 offen offset:0 sc1\n"
      "s_nop 6\n"
      "v_mfma_f32_16x16x32_bf16 v[80:83], v[104:107], v[116:119], v[80:83]\n"
      "buffer_load_dwordx4 v[116:119], %[vh], %[rs], s65 offen offset:0 sc1\n"
      "s_nop 6\n"
      "v_mfma_f32_16x16x32_bf16 v[84:87], v[104:107], v[120:123], v[84:87]\n"
      "buffer_load_dwordx4 v[120:123], v167, %[rs], s65 offen offset:0 sc1\n"
      "s_nop 6\n"
      "buffer_load_dwordx4 v[104:107], %[l16], %[rs], s63 offen offset:0 sc1\n"
      "s_add_u32 s60, s60, 1\n"
      "s_cmp_eq_u32 s60, 12\n"
      "s_cselect_b32 s60, 0, s60\n"
      "s_lshl_b32 s61, s60, 11\n"
      "s_add_u32 s65, s61, %[hb]\n"
      "s_add_u32 s61, s61, %[ha]\n"
      "s_mul_i32 s62, s60, 5120\n"
      "s_add_u32 s62, s62, %[wo]\n"
      "s_add_u32 s63, s62, 4096\n"
      "s_waitcnt vmcnt(12)\n"
      "v_mfma_f32_16x16x32_bf16 v[8:11], v[124:127], v[144:147], v[8:11]\n"
      "s_nop 6\n"
      "v_mfma_f32_16x16x32_bf16 v[12:15], v[124:127], v[148:151], v[12:15]\n"
      "s_nop 6\n"
      "v_mfma_f32_16x16x32_bf16 v[16:19], v[124:127], v[152:155], v[16:19]\n"
      "s_nop 6\n"
      "v_mfma_f32_16x16x32_bf16 v[20:23], v[124:127], v[156:159], v[20:23]\n"
      "s_nop 6\n"
      "buffer_load_dwordx4 v[124:127], %[l16], %[rs], s62 offen offset:0 sc1\n"
      "v_mfma_f32_16x16x32_bf16 v[24:27], v[128:131], v[144:147], v[24:27]\n"
      "s_nop 6\n"
      "v_mfma_f32_16x16x32_bf16 v[28:31], v[128:131], v[148:151], v[28:31]\n"
      "s_nop 6\n"
      "v_mfma_f32_16x16x32_bf16 v[32:35], v[128:131], v[152:155], v[32:35]\n"
      "s_nop 6\n"
      "v_mfma_f32_16x16x32_bf16 v[36:39], v[128:131], v[156:159], v[36:39]\n"
      "s_nop 6\n"
      "buffer_load_dwordx4 v[128:131], %[l16], %[rs], s62 offen offset:1024 sc1\n"
      "v_mfma_f32_16x16x32_bf16 v[40:43], v[132:135], v[144:147], v[40:43]\n"
      "s_nop 6\n"
      "v_mfma_f32_16x16x32_bf16 v[44:47], v[132:135], v[148:151], v[44:47]\n"
      "s_nop 6\n"
      "v_mfma_f32_16x16x32_bf16 v[48:51], v[132:135], v[152:155], v[48:51]\n"
      "s_nop 6\n"
      "v_mfma_f32_16x16x32_bf16 v[52:55], v[132:135], v[156:159], v[52:55]\n"
      "s_nop 6\n"
      "buffer_load_dwordx4 v[132:135], %[l16], %[rs], s62 offen offset:2048 sc1\n"
      "v_mfma_f32_16x16x32_bf16 v[56:59], v[136:139], v[144:147], v[56:59]\n"
      "s_nop 6\n"
      "v_mfma_f32_16x16x32_bf16 v[60:63], v[136:139], v[148:151], v[60:63]\n"
      "s_nop 6\n"
      "v_mfma_f32_16x16x32_bf16 v[64:67], v[136:139], v[152:155], v[64:67]\n"
      "s_nop 6\n"
      "v_mfma_f32_16x16x32_bf16 v[68:71], v[136:139], v[156:159], v[68:71]\n"
      "s_nop 6\n"
      "buffer_load_dwordx4 v[136:139], %[l16], %[rs], s62 offen offset:3072 sc1\n"
      "v_mfma_f32_16x16x32_bf16 v[72:75], v[140:143], v[144:147], v[72:75]\n"
      "buffer_load_dwordx4 v[144:147], %[vh], %[rs], s61 offen offset:0 sc1\n"
      "s_nop 6\n"
      "v_mfma_f32_16x16x32_bf16 v[76:79], v[140:143], v[148:151], v[76:79]\n"
      "buffer_load_dwordx4 v[148:151], v167, %[rs], s61 offen offset:0 sc1\n"
      "s_nop 6\n"
      "v_mfma_f32_16x16x32_bf16 v[80:83], v[140:143], v[152:155], v[80:83]\n"
      "buffer_load_dwordx4 v[152:155], %[vh], %[rs], s65 offen offset:0 sc1\n"
      "s_nop 6\n"
      "v_mfma_f32_16x16x32_bf16 v[84:87], v[140:143], v[156:159], v[84:87]\n"
      "buffer_load_dwordx4 v[156:159], v167, %[rs], s65 offen offset:0 sc1\n"
      "s_nop 6\n"
      "buffer_load_dwordx4 v[140:143], %[l16], %[rs], s63 offen offset:0 sc1\n"
      "s_xor_b32 s62, %[w], 0\n"
      "s_mul_i32 s62, s62, 15360\n"
      "s_add_u32 s62, s62, %[ex]\n"
      "v_add_u32 v162, s62, %[l16]\n"
      "s_xor_b32 s62, %[w], 1\n"
      "s_mul_i32 s62, s62, 15360\n"
      "s_add_u32 s62, s62, %[ex]\n"
      "v_add_u32 v163, s62, %[l16]\n"
      "s_xor_b32 s62, %[w], 2\n"
      "s_mul_i32 s62, s62, 15360\n"
      "s_add_u32 s62, s62, %[ex]\n"
      "v_add_u32 v164, s62, %[l16]\n"
      "s_xor_b32 s62, %[w], 3\n"
      "s_mul_i32 s62, s62, 15360\n"
      "s_add_u32 s62, s62, %[ex]\n"
      "v_add_u32 v165, s62, %[l16]\n"
      "s_waitcnt vmcnt(9)\n"
      "v_mfma_f32_16x16x32_bf16 v[12:15], v[88:91], v[112:115], v[12:15]\n"
      "s_nop 6\n"
      "v_mfma_f32_16x16x32_bf16 v[16:19], v[88:91], v[116:119], v[16:19]\n"
      "s_nop 6\n"
      "v_mfma_f32_16x16x32_bf16 v[20:23], v[88:91], v[120:123], v[20:23]\n"
      "s_nop 6\n"
      "v_mfma_f32_16x16x32_bf16 v[8:11], v[88:91], v[108:111], v[8:11]\n"
      "s_nop 6\n"
      "s_waitcnt vmcnt(0)\n"
      "v_mfma_f32_16x16x32_bf16 v[12:15], v[124:127], v[148:151], v[12:15]\n"
      "s_nop 6\n"
      "v_mfma_f32_16x16x32_bf16 v[16:19], v[124:127], v[152:155], v[16:19]\n"
      "s_nop 6\n"
      "v_mfma_f32_16x16x32_bf16 v[20:23], v[124:127], v[156:159], v[20:23]\n"
      "s_nop 6\n"
      "v_mfma_f32_16x16x32_bf16 v[8:11], v[124:127], v[144:147], v[8:11]\n"
      "s_nop 6\n"
      "s_setprio 3\n"
      "Lwb_%=:\n"
      "ds_read_b32 v160, %[ad]\n"
      "s_waitcnt lgkmcnt(0)\n"
      "v_cmp_gt_u32 vcc, 7, v160\n"
      "s_cbranch_vccz Lgb_%=\n"
      "s_sleep 2\n"
      "s_sub_u32 s66, s66, 1\n"
      "s_cbranch_scc0 Lwb_%=\n"
      "s_mov_b32 s66, 0\n"
      "Lgb_%=:\n"
      "s_setprio 3\n"
      "v_mfma_f32_16x16x32_bf16 v[28:31], v[92:95], v[112:115], v[28:31]\n"
      "s_nop 6\n"
      "v_mfma_f32_16x16x32_bf16 v[32:35], v[92:95], v[116:119], v[32:35]\n"
      "s_nop 6\n"
      "v_mfma_f32_16x16x32_bf16 v[36:39], v[92:95], v[120:123], v[36:39]\n"
      "s_nop 3\n"
      "ds_write_b128 v163, v[12:15] offset:0\n"
      "s_nop 6\n"
      "v_mfma_f32_16x16x32_bf16 v[24:27], v[92:95], v[108:111], v[24:27]\n"
      "s_nop 6\n"
      "v_mfma_f32_16x16x32_bf16 v[28:31], v[128:131], v[148:151], v[28:31]\n"
      "ds_write_b128 v164, v[16:19] offset:1024\n"
      "s_nop 6\n"
      "v_mfma_f32_16x16x32_bf16 v[32:35], v[128:131], v[152:155], v[32:35]\n"
      "s_nop 6\n"
      "v_mfma_f32_16x16x32_bf16 v[36:39], v[128:131], v[156:159], v[36:39]\n"
      "ds_write_b128 v165, v[20:23] offset:2048\n"
      "s_nop 6\n"
      "v_mfma_f32_16x16x32_bf16 v[24:27], v[128:131], v[144:147], v[24:27]\n"
      "s_nop 6\n"
      "v_mfma_f32_16x16x32_bf16 v[44:47], v[96:99], v[112:115], v[44:47]\n"
      "s_nop 6\n"
      "v_mfma_f32_16x16x32_bf16 v[48:51], v[96:99], v[116:119], v[48:51]\n"
      "s_nop 6\n"
      "v_mfma_f32_16x16x32_bf16 v[52:55], v[96:99], v[120:123], v[52:55]\n"
      "s_nop 3\n"
      "ds_write_b128 v163, v[28:31] offset:3072\n"
      "s_nop 6\n"
      "v_mfma_f32_16x16x32_bf16 v[40:43], v[96:99], v[108:111], v[40:43]\n"
      "s_nop 6\n"
      "v_mfma_f32_16x16x32_bf16 v[44:47], v[132:135], v[148:151], v[44:47]\n"
      "ds_write_b128 v164, v[32:35] offset:4096\n"
      "s_nop 6\n"
      "v_mfma_f32_16x16x32_bf16 v[48:51], v[132:135], v[152:155], v[48:51]\n"
      "s_nop 6\n"
      "v_mfma_f32_16x16x32_bf16 v[52:55], v[132:135], v[156:159], v[52:55]\n"
      "ds_write_b128 v165, v[36:39] offset:5120\n"
      "s_nop 6\n"
      "v_mfma_f32_16x16x32_bf16 v[40:43], v[132:135], v[144:147], v[40:43]\n"
      "s_nop 6\n"
      "v_mfma_f32_16x16x32_bf16 v[60:63], v[100:103], v[112:115], v[60:63]\n"
      "s_nop 6\n"
      "v_mfma_f32_16x16x32_bf16 v[64:67], v[100:103], v[116:119], v[64:67]\n"
      "s_nop 6\n"
      "v_mfma_f32_16x16x32_bf16 v[68:71], v[100:103], v[120:123], v[68:71]\n"
      "s_nop 3\n"
      "ds_write_b128 v163, v[44:47] offset:6144\n"
      "s_nop 6\n"
      "v_mfma_f32_16x16x32_bf16 v[56:59], v[100:103], v[108:111], v[56:59]\n"
      "s_nop 6\n"
      "v_mfma_f32_16x16x32_bf16 v[60:63], v[136:139], v[148:151], v[60:63]\n"
      "ds_write_b128 v164, v[48:51] offset:7168\n"
      "s_nop 6\n"
      "v_mfma_f32_16x16x32_bf16 v[64:67], v[136:139], v[152:155], v[64:67]\n"
      "s_nop 6\n"
      "v_mfma_f32_16x16x32_bf16 v[68:71], v[136:139], v[156:159], v[68:71]\n"
      "ds_write_b128 v165, v[52:55] offset:8192\n"
      "s_nop 6\n"
      "v_mfma_f32_16x16x32_bf16 v[56:59], v[136:139], v[144:147], v[56:59]\n"
      "s_nop 6\n"
      "v_mfma_f32_16x16x32_bf16 v[76:79], v[104:107], v[112:115], v[76:79]\n"
      "s_nop 6\n"
      "v_mfma_f32_16x16x32_bf16 v[80:83], v[104:107], v[116:119], v[80:83]\n"
      "s_nop 6\n"
      "v_mfma_f32_16x16x32_bf16 v[84:87], v[104:107], v[120:123], v[84:87]\n"
      "s_nop 3\n"
      "ds_write_b128 v163, v[60:63] offset:9216\n"
      "s_nop 6\n"
      "v_mfma_f32_16x16x32_bf16 v[72:75], v[104:107], v[108:111], v[72:75]\n"
      "s_nop 6\n"
      "v_mfma_f32_16x16x32_bf16 v[76:79], v[140:143], v[148:151], v[76:79]\n"
      "ds_write_b128 v164, v[64:67] offset:10240\n"
      "s_nop 6\n"
      "v_mfma_f32_16x16x32_bf16 v[80:83], v[140:143], v[152:155], v[80:83]\n"
      "s_nop 6\n"
      "v_mfma_f32_16x16x32_bf16 v[84:87], v[140:143], v[156:159], v[84:87]\n"
      "ds_write_b128 v165, v[68:71] offset:11264\n"
      "s_nop 6\n"
      "v_mfma_f32_16x16x32_bf16 v[72:75], v[140:143], v[144:147], v[72:75]\n"
      "s_nop 6\n"
      "s_setprio 3\n"
      "v_bfe_u32 v112, %[l16], 4, 4\n"
      "v_lshrrev_b32 v113, 8, %[l16]\n"
      "v_mul_u32_u24 v160, 1280, v112\n"
      "v_lshl_add_u32 v161, v113, 3, v160\n"
      "v_lshl_add_u32 v160, v113, 4, v160\n"
      "v_lshlrev_b32 v114, 4, v113\n"
      "v_lshlrev_b32 v115, 3, v113\n"
      "v_lshlrev_b32 v112, 3, v112\n"
      "global_load_dwordx4 v[88:91], v160, %[xp]\n"
      "global_load_dwordx4 v[92:95], v160, %[xp] offset:64\n"
      "global_load_dwordx2 v[96:97], v161, %[xp] offset:128\n"
      "global_load_dwordx4 v[98:101], v114, %[bp]\n"
      "global_load_dwordx4 v[102:105], v114, %[bp] offset:64\n"
      "global_load_dwordx2 v[106:107], v115, %[bp] offset:128\n"
      "buffer_load_dword v166, v112, %[rs], %[ro] offen sc1\n"
      "ds_write_b128 v163, v[76:79] offset:12288\n"
      "s_sleep 1\n"
      "s_nop 7\n"
      "ds_write_b128 v164, v[80:83] offset:13312\n"
      "s_sleep 1\n"
      "s_nop 7\n"
      "ds_write_b128 v165, v[84:87] offset:14336\n"
      "v_mov_b32 v117, 3\n"
      "s_waitcnt lgkmcnt(0)\n"
      "ds_write_b32 %[sgw], v117\n"
      "Lwc_%=:\n"
      "ds_read_b32 v116, %[sgr]\n"
      "s_waitcnt lgkmcnt(0)\n"
      "v_cmp_gt_u32 vcc, 3, v116\n"
      "s_cbranch_vccz Lgc_%=\n"
      "s_sleep 2\n"
      "s_sub_u32 s66, s66, 1\n"
      "s_cbranch_scc0 Lwc_%=\n"
      "s_mov_b32 s66, 0\n"
      "Lgc_%=:\n"
      "ds_read_b128 v[12:15], v162 offset:0\n"
      "ds_read_b128 v[16:19], v162 offset:1024\n"
      "ds_read_b128 v[20:23], v162 offset:2048\n"
      "s_sleep 1\n"
      "ds_read_b128 v[28:31], v162 offset:3072\n"
      "ds_read_b128 v[32:35], v162 offset:4096\n"
      "ds_read_b128 v[36:39], v162 offset:5120\n"
      "s_sleep 1\n"
      "ds_read_b128 v[44:47], v162 offset:6144\n"
      "ds_read_b128 v[48:51], v162 offset:7168\n"
      "ds_read_b128 v[52:55], v162 offset:8192\n"
      "s_sleep 1\n"
      "ds_read_b128 v[60:63], v162 offset:9216\n"
      "ds_read_b128 v[64:67], v162 offset:10240\n"
      "ds_read_b128 v[68:71], v162 offset:11264\n"
      "s_sleep 1\n"
      "ds_read_b128 v[76:79], v162 offset:12288\n"
      "ds_read_b128 v[80:83], v162 offset:13312\n"
      "ds_read_b128 v[84:87], v162 offset:14336\n"
      "s_sleep 1\n"
      "v_mov_b32 v117, 4\n"
      "s_waitcnt lgkmcnt(0)\n"
      "ds_write_b32 %[sgw], v117\n"
      "Lwd_%=:\n"
      "ds_read_b32 v116, %[sgr]\n"
      "s_waitcnt lgkmcnt(0)\n"
      "v_cmp_gt_u32 vcc, 4, v116\n"
      "s_cbranch_vccz Lgd_%=\n"
      "s_sleep 2\n"
      "s_sub_u32 s66, s66, 1\n"
      "s_cbranch_scc0 Lwd_%=\n"
      "s_mov_b32 s66, 0\n"
      "Lgd_%=:\n"
      "s_add_u32 m0, %[lw], 49152\n"
      "s_add_u32 s64, %[fw], 49152\n"
      "buffer_load_dwordx4 %[l16], %[rs], s64 offen sc1 lds\n"
      "s_add_u32 m0, %[lw], 53248\n"
      "s_add_u32 s64, %[fw], 53248\n"
      "buffer_load_dwordx4 %[l16], %[rs], s64 offen sc1 lds\n"
      "s_add_u32 m0, %[lw], 57344\n"
      "s_add_u32 s64, %[fw], 57344\n"
      "buffer_load_dwordx4 %[l16], %[rs], s64 offen sc1 lds\n"
      "s_add_u32 m0, %[lw], 61440\n"
      "s_add_u32 s64, %[fw], 61440\n"
      "buffer_load_dwordx4 %[l16], %[rs], s64 offen sc1 lds\n"
      "s_add_u32 m0, %[lw], 65536\n"
      "s_add_u32 s64, %[fw], 65536\n"
      "buffer_load_dwordx4 %[l16], %[rs], s64 offen sc1 lds\n"
      "s_add_u32 m0, %[lw], 69632\n"
      "s_add_u32 s64, %[fw], 69632\n"
      "buffer_load_dwordx4 %[l16], %[rs], s64 offen sc1 lds\n"
      "s_add_u32 m0, %[lw], 73728\n"
      "s_add_u32 s64, %[fw], 73728\n"
      "buffer_load_dwordx4 %[l16], %[rs], s64 offen sc1 lds\n"
      "s_add_u32 m0, %[lw], 77824\n"
      "s_add_u32 s64, %[fw], 77824\n"
      "buffer_load_dwordx4 %[l16], %[rs], s64 offen sc1 lds\n"
      "s_add_u32 m0, %[lw], 81920\n"
      "s_add_u32 s64, %[fw], 81920\n"
      "buffer_load_dwordx4 %[l16], %[rs], s64 offen sc1 lds\n"
      "s_add_u32 m0, %[lw], 86016\n"
      "s_add_u32 s64, %[fw], 86016\n"
      "buffer_load_dwordx4 %[l16], %[rs], s64 offen sc1 lds\n"
      "s_add_u32 m0, %[lw], 90112\n"
      "s_add_u32 s64, %[fw], 90112\n"
      "buffer_load_dwordx4 %[l16], %[rs], s64 offen sc1 lds\n"
      "s_add_u32 m0, %[lw], 94208\n"
      "s_add_u32 s64, %[fw], 94208\n"
      "buffer_load_dwordx4 %[l16], %[rs], s64 offen sc1 lds\n"
      "s_add_u32 m0, %[lw], 98304\n"
      "s_add_u32 s64, %[fw], 98304\n"
      "buffer_load_dwordx4 %[l16], %[rs], s64 offen sc1 lds\n"
      "s_setprio 0\n"
      "v_add_f32 v8, v8, v12\n"
      "v_add_f32 v9, v9, v13\n"
      "v_add_f32 v10, v10, v14\n"
      "v_add_f32 v11, v11, v15\n"
      "v_add_f32 v16, v16, v20\n"
      "v_add_f32 v17, v17, v21\n"
      "v_add_f32 v18, v18, v22\n"
      "v_add_f32 v19, v19, v23\n"
      "v_add_f32 v140, v8, v16\n"
      "v_add_f32 v141, v9, v17\n"
      "v_add_f32 v142, v10, v18\n"
      "v_add_f32 v143, v11, v19\n"
      "v_add_f32 v24, v24, v28\n"
      "v_add_f32 v25, v25, v29\n"
      "v_add_f32 v26, v26, v30\n"
      "v_add_f32 v27, v27, v31\n"
      "v_add_f32 v32, v32, v36\n"
      "v_add_f32 v33, v33, v37\n"
      "v_add_f32 v34, v34, v38\n"
      "v_add_f32 v35, v35, v39\n"
      "v_add_f32 v144, v24, v32\n"
      "v_add_f32 v145, v25, v33\n"
      "v_add_f32 v146, v26, v34\n"
      "v_add_f32 v147, v27, v35\n"
      "v_add_f32 v40, v40, v44\n"
      "v_add_f32 v41, v41, v45\n"
      "v_add_f32 v42, v42, v46\n"
      "v_add_f32 v43, v43, v47\n"
      "v_add_f32 v48, v48, v52\n"
      "v_add_f32 v49, v49, v53\n"
      "v_add_f32 v50, v50, v54\n"
      "v_add_f32 v51, v51, v55\n"
      "v_add_f32 v148, v40, v48\n"
      "v_add_f32 v149, v41, v49\n"
      "v_add_f32 v150, v42, v50\n"
      "v_add_f32 v151, v43, v51\n"
      "v_add_f32 v56, v56, v60\n"
      "v_add_f32 v57, v57, v61\n"
      "v_add_f32 v58, v58, v62\n"
      "v_add_f32 v59, v59, v63\n"
      "v_add_f32 v64, v64, v68\n"
      "v_add_f32 v65, v65, v69\n"
      "v_add_f32 v66, v66, v70\n"
      "v_add_f32 v67, v67, v71\n"
      "v_add_f32 v152, v56, v64\n"
      "v_add_f32 v153, v57, v65\n"
      "v_add_f32 v154, v58, v66\n"
      "v_add_f32 v155, v59, v67\n"
      "v_add_f32 v72, v72, v76\n"
      "v_add_f32 v73, v73, v77\n"
      "v_add_f32 v74, v74, v78\n"
      "v_add_f32 v75, v75, v79\n"
      "v_add_f32 v80, v80, v84\n"
      "v_add_f32 v81, v81, v85\n"
      "v_add_f32 v82, v82, v86\n"
      "v_add_f32 v83, v83, v87\n"
      "v_add_f32 v156, v72, v80\n"
      "v_add_f32 v157, v73, v81\n"
      "v_add_f32 v158, v74, v82\n"
      "v_add_f32 v159, v75, v83\n"
      "v_add_u32 v162, %[wb], %[l16]\n"
      "v_add_u32 v163, 51200, v162\n"
      "v_and_b32 v164, 0x1ff, %[vh]\n"
      "v_lshrrev_b32 v164, 1, v164\n"
      "v_and_b32 v165, 0x600, %[vh]\n"
      "v_add_u32 v164, v164, v165\n"
      "s_mov_b32 s67, 0x100000\n"
      "s_waitcnt vmcnt(13)\n"
      "v_mul_f32 v166, v166, v166\n"
      "v_lshlrev_b32 v68, 16, v98\n"
      "v_and_b32 v69, 0xffff0000, v98\n"
      "v_lshlrev_b32 v70, 16, v99\n"
      "v_and_b32 v71, 0xffff0000, v99\n"
      "v_fma_f32 v140, v140, v166, v68\n"
      "v_fma_f32 v141, v141, v166, v69\n"
      "v_fma_f32 v142, v142, v166, v70\n"
      "v_fma_f32 v143, v143, v166, v71\n"
      "v_mul_f32 v68, 0xbfb8aa3b, v140\n"
      "v_mul_f32 v69, 0xbfb8aa3b, v141\n"
      "v_mul_f32 v70, 0xbfb8aa3b, v142\n"
      "v_mul_f32 v71, 0xbfb8aa3b, v143\n"
      "v_exp_f32 v68, v68\n"
      "v_exp_f32 v69, v69\n"
      "v_exp_f32 v70, v70\n"
      "v_exp_f32 v71, v71\n"
      "s_nop 0\n"
      "v_add_f32 v68, 1.0, v68\n"
      "v_add_f32 v69, 1.0, v69\n"
      "v_add_f32 v70, 1.0, v70\n"
      "v_add_f32 v71, 1.0, v71\n"
      "v_rcp_f32 v68, v68\n"
      "v_rcp_f32 v69, v69\n"
      "v_rcp_f32 v70, v70\n"
      "v_rcp_f32 v71, v71\n"
      "s_nop 0\n"
      "v_mul_f32 v140, v140, v68\n"
      "v_mul_f32 v141, v141, v69\n"
      "v_mul_f32 v142, v142, v70\n"
      "v_mul_f32 v143, v143, v71\n"
      "v_lshlrev_b32 v68, 16, v100\n"
      "v_and_b32 v69, 0xffff0000, v100\n"
      "v_lshlrev_b32 v70, 16, v101\n"
      "v_and_b32 v71, 0xffff0000, v101\n"
      "v_fma_f32 v144, v144, v166, v68\n"
      "v_fma_f32 v145, v145, v166, v69\n"
      "v_fma_f32 v146, v146, v166, v70\n"
      "v_fma_f32 v147, v147, v166, v71\n"
      "v_mul_f32 v68, 0xbfb8aa3b, v144\n"
      "v_mul_f32 v69, 0xbfb8aa3b, v145\n"
      "v_mul_f32 v70, 0xbfb8aa3b, v146\n"
      "v_mul_f32 v71, 0xbfb8aa3b, v147\n"
      "v_exp_f32 v68, v68\n"
      "v_exp_f32 v69, v69\n"
      "v_exp_f32 v70, v70\n"
      "v_exp_f32 v71, v71\n"
      "s_nop 0\n"
      "v_add_f32 v68, 1.0, v68\n"
      "v_add_f32 v69, 1.0, v69\n"
      "v_add_f32 v70, 1.0, v70\n"
      "v_add_f32 v71, 1.0, v71\n"
      "v_rcp_f32 v68, v68\n"
      "v_rcp_f32 v69, v69\n"
      "v_rcp_f32 v70, v70\n"
      "v_rcp_f32 v71, v71\n"
      "s_nop 0\n"
      "v_mul_f32 v144, v144, v68\n"
      "v_mul_f32 v145, v145, v69\n"
      "v_mul_f32 v146, v146, v70\n"
      "v_mul_f32 v147, v147, v71\n"
      "v_lshlrev_b32 v68, 16, v102\n"
      "v_and_b32 v69, 0xffff0000, v102\n"
      "v_lshlrev_b32 v70, 16, v103\n"
      "v_and_b32 v71, 0xffff0000, v103\n"
      "v_fma_f32 v148, v148, v166, v68\n"
      "v_fma_f32 v149, v149, v166, v69\n"
      "v_fma_f32 v150, v150, v166, v70\n"
      "v_fma_f32 v151, v151, v166, v71\n"
      "v_mul_f32 v68, 0xbfb8aa3b, v148\n"
      "v_mul_f32 v69, 0xbfb8aa3b, v149\n"
      "v_mul_f32 v70, 0xbfb8aa3b, v150\n"
      "v_mul_f32 v71, 0xbfb8aa3b, v151\n"
      "v_exp_f32 v68, v68\n"
      "v_exp_f32 v69, v69\n"
      "v_exp_f32 v70, v70\n"
      "v_exp_f32 v71, v71\n"
      "s_nop 0\n"
      "v_add_f32 v68, 1.0, v68\n"
      "v_add_f32 v69, 1.0, v69\n"
      "v_add_f32 v70, 1.0, v70\n"
      "v_add_f32 v71, 1.0, v71\n"
      "v_rcp_f32 v68, v68\n"
      "v_rcp_f32 v69, v69\n"
      "v_rcp_f32 v70, v70\n"
      "v_rcp_f32 v71, v71\n"
      "s_nop 0\n"
      "v_mul_f32 v148, v148, v68\n"
      "v_mul_f32 v149, v149, v69\n"
      "v_mul_f32 v150, v150, v70\n"
      "v_mul_f32 v151, v151, v71\n"
      "v_mov_b32 v167, 5\n"
      "s_waitcnt vmcnt(0)\n"
      "ds_write_b32 %[sgw], v167\n"
      "s_mul_i32 s63, %[par], 0\n"
      "s_sub_u32 s63, 9, s63\n"
      "Lwf_%=:\n"
      "ds_read_b32 v165, %[ad]\n"
      "s_waitcnt lgkmcnt(0)\n"
      "v_cmp_gt_u32 vcc, s63, v165\n"
      "s_cbranch_vccz Lgf_%=\n"
      "s_sleep 2\n"
      "s_sub_u32 s66, s66, 1\n"
      "s_cbranch_scc0 Lwf_%=\n"
      "s_mov_b32 s66, 0\n"
      "Lgf_%=:\n"
      "s_cmp_eq_u32 %[par], 1\n"
      "s_cbranch_scc1 Lpz%=\n"
      "s_sleep 14\n"
      "Lpz%=:\n"
      "s_sleep 2\n"
      "s_add_u32 s62, %[ob], 0\n"
      "buffer_load_dwordx4 v[108:111], %[vh], %[rs], s62 offen offset:0 sc1\n"
      "s_add_u32 s62, %[ob], 2048\n"
      "buffer_load_dwordx4 v[112:115], %[vh], %[rs], s62 offen offset:0 sc1\n"
      "s_add_u32 s62, %[ob], 10240\n"
      "buffer_load_dwordx4 v[116:119], %[vh], %[rs], s62 offen offset:0 sc1\n"
      "s_add_u32 s62, %[ob], 12288\n"
      "buffer_load_dwordx4 v[120:123], %[vh], %[rs], s62 offen offset:0 sc1\n"
      "s_add_u32 s62, %[ob], 20480\n"
      "buffer_load_dwordx4 v[124:127], %[vh], %[rs], s62 offen offset:0 sc1\n"
      "s_add_u32 s62, %[ob], 22528\n"
      "buffer_load_dwordx4 v[128:131], %[vh], %[rs], s62 offen offset:0 sc1\n"
      "Lwe_%=:\n"
      "ds_read_b32 v165, %[sgr]\n"
      "s_waitcnt lgkmcnt(0)\n"
      "v_cmp_gt_u32 vcc, 5, v165\n"
      "s_cbranch_vccz Lge_%=\n"
      "s_sleep 2\n"
      "s_sub_u32 s66, s66, 1\n"
      "s_cbranch_scc0 Lwe_%=\n"
      "s_mov_b32 s66, 0\n"
      "Lge_%=:\n"
      "ds_read_b128 v[8:11], v162 offset:0\n"
      "ds_read_b128 v[12:15], v162 offset:1024\n"
      "ds_read_b128 v[16:19], v162 offset:2048\n"
      "ds_read_b128 v[20:23], v162 offset:3072\n"
      "ds_read_b128 v[24:27], v162 offset:4096\n"
      "ds_read_b128 v[28:31], v162 offset:5120\n"
      "ds_read_b128 v[32:35], v162 offset:6144\n"
      "ds_read_b128 v[36:39], v162 offset:7168\n"
      "ds_read_b128 v[40:43], v162 offset:8192\n"
      "ds_read_b128 v[44:47], v162 offset:9216\n"
      "ds_read_b128 v[48:51], v162 offset:25600\n"
      "ds_read_b128 v[52:55], v162 offset:26624\n"
      "ds_read_b128 v[56:59], v162 offset:27648\n"
      "ds_read_b128 v[60:63], v162 offset:28672\n"
      "ds_read_b128 v[64:67], v162 offset:29696\n"
      "v_lshlrev_b32 v68, 16, v104\n"
      "v_and_b32 v69, 0xffff0000, v104\n"
      "v_lshlrev_b32 v70, 16, v105\n"
      "v_and_b32 v71, 0xffff0000, v105\n"
      "v_fma_f32 v152, v152, v166, v68\n"
      "v_fma_f32 v153, v153, v166, v69\n"
      "v_fma_f32 v154, v154, v166, v70\n"
      "v_fma_f32 v155, v155, v166, v71\n"
      "v_mul_f32 v68, 0xbfb8aa3b, v152\n"
      "v_mul_f32 v69, 0xbfb8aa3b, v153\n"
      "v_mul_f32 v70, 0xbfb8aa3b, v154\n"
      "v_mul_f32 v71, 0xbfb8aa3b, v155\n"
      "v_exp_f32 v68, v68\n"
      "v_exp_f32 v69, v69\n"
      "v_exp_f32 v70, v70\n"
      "v_exp_f32 v71, v71\n"
      "s_nop 0\n"
      "v_add_f32 v68, 1.0, v68\n"
      "v_add_f32 v69, 1.0, v69\n"
      "v_add_f32 v70, 1.0, v70\n"
      "v_add_f32 v71, 1.0, v71\n"
      "v_rcp_f32 v68, v68\n"
      "v_rcp_f32 v69, v69\n"
      "v_rcp_f32 v70, v70\n"
      "v_rcp_f32 v71, v71\n"
      "s_nop 0\n"
      "v_mul_f32 v152, v152, v68\n"
      "v_mul_f32 v153, v153, v69\n"
      "v_mul_f32 v154, v154, v70\n"
      "v_mul_f32 v155, v155, v71\n"
      "v_lshlrev_b32 v68, 16, v106\n"
      "v_and_b32 v69, 0xffff0000, v106\n"
      "v_lshlrev_b32 v70, 16, v107\n"
      "v_and_b32 v71, 0xffff0000, v107\n"
      "v_fma_f32 v156, v156, v166, v68\n"
      "v_fma_f32 v157, v157, v166, v69\n"
      "v_fma_f32 v158, v158, v166, v70\n"
      "v_fma_f32 v159, v159, v166, v71\n"
      "v_mul_f32 v68, 0xbfb8aa3b, v156\n"
      "v_mul_f32 v69, 0xbfb8aa3b, v157\n"
      "v_mul_f32 v70, 0xbfb8aa3b, v158\n"
      "v_mul_f32 v71, 0xbfb8aa3b, v159\n"
      "v_exp_f32 v68, v68\n"
      "v_exp_f32 v69, v69\n"
      "v_exp_f32 v70, v70\n"
      "v_exp_f32 v71, v71\n"
      "s_nop 0\n"
      "v_add_f32 v68, 1.0, v68\n"
      "v_add_f32 v69, 1.0, v69\n"
      "v_add_f32 v70, 1.0, v70\n"
      "v_add_f32 v71, 1.0, v71\n"
      "v_rcp_f32 v68, v68\n"
      "v_rcp_f32 v69, v69\n"
      "v_rcp_f32 v70, v70\n"
      "v_rcp_f32 v71, v71\n"
      "s_nop 0\n"
      "v_mul_f32 v156, v156, v68\n"
      "v_mul_f32 v157, v157, v69\n"
      "v_mul_f32 v158, v158, v70\n"
      "v_mul_f32 v159, v159, v71\n"
      "v_lshlrev_b32 v98, 16, v88\n"
      "v_and_b32 v88, 0xffff0000, v88\n"
      "v_lshlrev_b32 v99, 16, v89\n"
      "v_and_b32 v89, 0xffff0000, v89\n"
      "v_lshlrev_b32 v100, 16, v90\n"
      "v_and_b32 v90, 0xffff0000, v90\n"
      "v_lshlrev_b32 v101, 16, v91\n"
      "v_and_b32 v91, 0xffff0000, v91\n"
      "v_lshlrev_b32 v102, 16, v92\n"
      "v_and_b32 v92, 0xffff0000, v92\n"
      "v_lshlrev_b32 v103, 16, v93\n"
      "v_and_b32 v93, 0xffff0000, v93\n"
      "v_lshlrev_b32 v104, 16, v94\n"
      "v_and_b32 v94, 0xffff0000, v94\n"
      "v_lshlrev_b32 v105, 16, v95\n"
      "v_and_b32 v95, 0xffff0000, v95\n"
      "v_lshlrev_b32 v106, 16, v96\n"
      "v_and_b32 v96, 0xffff0000, v96\n"
      "v_lshlrev_b32 v107, 16, v97\n"
      "v_and_b32 v97, 0xffff0000, v97\n"
      "s_waitcnt vmcnt(5)\n"
      "v_max3_u32 v165, v108, v109, v110\n"
      "v_max_u32 v165, v165, v111\n"
      "v_cmp_eq_u32 vcc, -1, v165\n"
      "s_cbranch_vccnz Lr0_%=\n"
      "Lo0_%=:\n"
      "s_waitcnt lgkmcnt(10)\n"
      "v_mfma_f32_16x16x32_bf16 v[68:71], v[8:11], v[108:111], 0\n"
      "ds_read_b128 v[8:11], v162 offset:30720\n"
      "v_mfma_f32_16x16x32_bf16 v[72:75], v[12:15], v[108:111], 0\n"
      "ds_read_b128 v[12:15], v162 offset:31744\n"
      "s_waitcnt vmcnt(4)\n"
      "v_max3_u32 v165, v112, v113, v114\n"
      "v_mfma_f32_16x16x32_bf16 v[76:79], v[16:19], v[108:111], 0\n"
      "ds_read_b128 v[16:19], v162 offset:32768\n"
      "v_max_u32 v165, v165, v115\n"
      "v_mfma_f32_16x16x32_bf16 v[80:83], v[20:23], v[108:111], 0\n"
      "ds_read_b128 v[20:23], v162 offset:33792\n"
      "v_cmp_eq_u32 vcc, -1, v165\n"
      "v_mfma_f32_16x16x32_bf16 v[84:87], v[24:27], v[108:111], 0\n"
      "ds_read_b128 v[24:27], v162 offset:34816\n"
      "s_add_u32 s62, %[ob], 30720\n"
      "buffer_load_dwordx4 v[108:111], %[vh], %[rs], s62 offen offset:0 sc1\n"
      "s_cbranch_vccnz Lr1_%=\n"
      "Lo1_%=:\n"
      "s_waitcnt lgkmcnt(10)\n"
      "v_mfma_f32_16x16x32_bf16 v[68:71], v[28:31], v[112:115], v[68:71]\n"
      "ds_read_b128 v[28:31], v163 offset:0\n"
      "v_mfma_f32_16x16x32_bf16 v[72:75], v[32:35], v[112:115], v[72:75]\n"
      "ds_read_b128 v[32:35], v163 offset:1024\n"
      "s_waitcnt vmcnt(4)\n"
      "v_max3_u32 v165, v116, v117, v118\n"
      "v_mfma_f32_16x16x32_bf16 v[76:79], v[36:39], v[112:115], v[76:79]\n"
      "ds_read_b128 v[36:39], v163 offset:2048\n"
      "v_max_u32 v165, v165, v119\n"
      "v_mfma_f32_16x16x32_bf16 v[80:83], v[40:43], v[112:115], v[80:83]\n"
      "ds_read_b128 v[40:43], v163 offset:3072\n"
      "v_cmp_eq_u32 vcc, -1, v165\n"
      "v_mfma_f32_16x16x32_bf16 v[84:87], v[44:47], v[112:115], v[84:87]\n"
      "ds_read_b128 v[44:47], v163 offset:4096\n"
      "s_add_u32 s62, %[ob], 32768\n"
      "buffer_load_dwordx4 v[112:115], %[vh], %[rs], s62 offen offset:0 sc1\n"
      "s_cbranch_vccnz Lr2_%=\n"
      "Lo2_%=:\n"
      "s_waitcnt lgkmcnt(10)\n"
      "v_mfma_f32_16x16x32_bf16 v[68:71], v[48:51], v[116:119], v[68:71]\n"
      "ds_read_b128 v[48:51], v163 offset:5120\n"
      "v_mfma_f32_16x16x32_bf16 v[72:75], v[52:55], v[116:119], v[72:75]\n"
      "ds_read_b128 v[52:55], v163 offset:6144\n"
      "s_waitcnt vmcnt(4)\n"
      "v_max3_u32 v165, v120, v121, v122\n"
      "v_mfma_f32_16x16x32_bf16 v[76:79], v[56:59], v[116:119], v[76:79]\n"
      "ds_read_b128 v[56:59], v163 offset:7168\n"
      "v_max_u32 v165, v165, v123\n"
      "v_mfma_f32_16x16x32_bf16 v[80:83], v[60:63], v[116:119], v[80:83]\n"
      "ds_read_b128 v[60:63], v163 offset:8192\n"
      "v_cmp_eq_u32 vcc, -1, v165\n"
      "v_mfma_f32_16x16x32_bf16 v[84:87], v[64:67], v[116:119], v[84:87]\n"
      "ds_read_b128 v[64:67], v163 offset:9216\n"
      "s_add_u32 s62, %[ob], 4096\n"
      "buffer_load_dwordx4 v[116:119], %[vh], %[rs], s62 offen offset:0 sc1\n"
      "s_cbranch_vccnz Lr3_%=\n"
      "Lo3_%=:\n"
      "s_waitcnt lgkmcnt(10)\n"
      "v_mfma_f32_16x16x32_bf16 v[68:71], v[8:11], v[120:123], v[68:71]\n"
      "ds_read_b128 v[8:11], v163 offset:25600\n"
      "v_mfma_f32_16x16x32_bf16 v[72:75], v[12:15], v[120:123], v[72:75]\n"
      "ds_read_b128 v[12:15], v163 offset:26624\n"
      "s_waitcnt vmcnt(4)\n"
      "v_max3_u32 v165, v124, v125, v126\n"
      "v_mfma_f32_16x16x32_bf16 v[76:79], v[16:19], v[120:123], v[76:79]\n"
      "ds_read_b128 v[16:19], v163 offset:27648\n"
      "v_max_u32 v165, v165, v127\n"
      "v_mfma_f32_16x16x32_bf16 v[80:83], v[20:23], v[120:123], v[80:83]\n"
      "ds_read_b128 v[20:23], v163 offset:28672\n"
      "v_cmp_eq_u32 vcc, -1, v165\n"
      "v_mfma_f32_16x16x32_bf16 v[84:87], v[24:27], v[120:123], v[84:87]\n"
      "ds_read_b128 v[24:27], v163 offset:29696\n"
      "s_add_u32 s62, %[ob], 6144\n"
      "buffer_load_dwordx4 v[120:123], %[vh], %[rs], s62 offen offset:0 sc1\n"
      "s_cbranch_vccnz Lr4_%=\n"
      "Lo4_%=:\n"
      "s_waitcnt lgkmcnt(10)\n"
      "v_mfma_f32_16x16x32_bf16 v[68:71], v[28:31], v[124:127], v[68:71]\n"
      "ds_read_b128 v[28:31], v163 offset:30720\n"
      "v_mfma_f32_16x16x32_bf16 v[72:75], v[32:35], v[124:127], v[72:75]\n"
      "ds_read_b128 v[32:35], v163 offset:31744\n"
      "s_waitcnt vmcnt(4)\n"
      "v_max3_u32 v165, v128, v129, v130\n"
      "v_mfma_f32_16x16x32_bf16 v[76:79], v[36:39], v[124:127], v[76:79]\n"
      "ds_read_b128 v[36:39], v163 offset:32768\n"
      "v_max_u32 v165, v165, v131\n"
      "v_mfma_f32_16x16x32_bf16 v[80:83], v[40:43], v[124:127], v[80:83]\n"
      "ds_read_b128 v[40:43], v163 offset:33792\n"
      "v_cmp_eq_u32 vcc, -1, v165\n"
      "v_mfma_f32_16x16x32_bf16 v[84:87], v[44:47], v[124:127], v[84:87]\n"
      "ds_read_b128 v[44:47], v163 offset:34816\n"
      "s_add_u32 s62, %[ob], 14336\n"
      "buffer_load_dwordx4 v[124:127], %[vh], %[rs], s62 offen offset:0 sc1\n"
      "s_cbranch_vccnz Lr5_%=\n"
      "Lo5_%=:\n"
      "s_waitcnt lgkmcnt(10)\n"
      "v_mfma_f32_16x16x32_bf16 v[68:71], v[48:51], v[128:131], v[68:71]\n"
      "ds_read_b128 v[48:51], v162 offset:10240\n"
      "v_mfma_f32_16x16x32_bf16 v[72:75], v[52:55], v[128:131], v[72:75]\n"
      "ds_read_b128 v[52:55], v162 offset:11264\n"
      "s_waitcnt vmcnt(4)\n"
      "v_max3_u32 v165, v108, v109, v110\n"
      "v_mfma_f32_16x16x32_bf16 v[76:79], v[56:59], v[128:131], v[76:79]\n"
      "ds_read_b128 v[56:59], v162 offset:12288\n"
      "v_max_u32 v165, v165, v111\n"
      "v_mfma_f32_16x16x32_bf16 v[80:83], v[60:63], v[128:131], v[80:83]\n"
      "ds_read_b128 v[60:63], v162 offset:13312\n"
      "v_cmp_eq_u32 vcc, -1, v165\n"
      "v_mfma_f32_16x16x32_bf16 v[84:87], v[64:67], v[128:131], v[84:87]\n"
      "ds_read_b128 v[64:67], v162 offset:14336\n"
      "s_add_u32 s62, %[ob], 16384\n"
      "buffer_load_dwordx4 v[128:131], %[vh], %[rs], s62 offen offset:0 sc1\n"
      "s_cbranch_vccnz Lr6_%=\n"
      "Lo6_%=:\n"
      "s_waitcnt lgkmcnt(10)\n"
      "v_mfma_f32_16x16x32_bf16 v[68:71], v[8:11], v[108:111], v[68:71]\n"
      "ds_read_b128 v[8:11], v162 offset:15360\n"
      "v_mfma_f32_16x16x32_bf16 v[72:75], v[12:15], v[108:111], v[72:75]\n"
      "ds_read_b128 v[12:15], v162 offset:16384\n"
      "s_waitcnt vmcnt(4)\n"
      "v_max3_u32 v165, v112, v113, v114\n"
      "v_mfma_f32_16x16x32_bf16 v[76:79], v[16:19], v[108:111], v[76:79]\n"
      "ds_read_b128 v[16:19], v162 offset:17408\n"
      "v_max_u32 v165, v165, v115\n"
      "v_mfma_f32_16x16x32_bf16 v[80:83], v[20:23], v[108:111], v[80:83]\n"
      "ds_read_b128 v[20:23], v162 offset:18432\n"
      "v_cmp_eq_u32 vcc, -1, v165\n"
      "v_mfma_f32_16x16x32_bf16 v[84:87], v[24:27], v[108:111], v[84:87]\n"
      "ds_read_b128 v[24:27], v162 offset:19456\n"
      "s_add_u32 s62, %[ob], 24576\n"
      "buffer_load_dwordx4 v[108:111], %[vh], %[rs], s62 offen offset:0 sc1\n"
      "s_cbranch_vccnz Lr7_%=\n"
      "Lo7_%=:\n"
      "s_waitcnt lgkmcnt(10)\n"
      "v_mfma_f32_16x16x32_bf16 v[68:71], v[28:31], v[112:115], v[68:71]\n"
      "ds_read_b128 v[28:31], v162 offset:35840\n"
      "v_mfma_f32_16x16x32_bf16 v[72:75], v[32:35], v[112:115], v[72:75]\n"
      "ds_read_b128 v[32:35], v162 offset:36864\n"
      "s_waitcnt vmcnt(4)\n"
      "v_max3_u32 v165, v116, v117, v118\n"
      "v_mfma_f32_16x16x32_bf16 v[76:79], v[36:39], v[112:115], v[76:79]\n"
      "ds_read_b128 v[36:39], v162 offset:37888\n"
      "v_max_u32 v165, v165, v119\n"
      "v_mfma_f32_16x16x32_bf16 v[80:83], v[40:43], v[112:115], v[80:83]\n"
      "ds_read_b128 v[40:43], v162 offset:38912\n"
      "v_cmp_eq_u32 vcc, -1, v165\n"
      "v_mfma_f32_16x16x32_bf16 v[84:87], v[44:47], v[112:115], v[84:87]\n"
      "ds_read_b128 v[44:47], v162 offset:39936\n"
      "s_add_u32 s62, %[ob], 26624\n"
      "buffer_load_dwordx4 v[112:115], %[vh], %[rs], s62 offen offset:0 sc1\n"
      "s_cbranch_vccnz Lr8_%=\n"
      "Lo8_%=:\n"
      "s_waitcnt lgkmcnt(10)\n"
      "v_mfma_f32_16x16x32_bf16 v[68:71], v[48:51], v[116:119], v[68:71]\n"
      "ds_read_b128 v[48:51], v162 offset:40960\n"
      "v_mfma_f32_16x16x32_bf16 v[72:75], v[52:55], v[116:119], v[72:75]\n"
      "ds_read_b128 v[52:55], v162 offset:41984\n"
      "s_waitcnt vmcnt(4)\n"
      "v_max3_u32 v165, v120, v121, v122\n"
      "v_mfma_f32_16x16x32_bf16 v[76:79], v[56:59], v[116:119], v[76:79]\n"
      "ds_read_b128 v[56:59], v162 offset:43008\n"
      "v_max_u32 v165, v165, v123\n"
      "v_mfma_f32_16x16x32_bf16 v[80:83], v[60:63], v[116:119], v[80:83]\n"
      "ds_read_b128 v[60:63], v162 offset:44032\n"
      "v_cmp_eq_u32 vcc, -1, v165\n"
      "v_mfma_f32_16x16x32_bf16 v[84:87], v[64:67], v[116:119], v[84:87]\n"
      "ds_read_b128 v[64:67], v162 offset:45056\n"
      "s_add_u32 s62, %[ob], 34816\n"
      "buffer_load_dwordx4 v[116:119], %[vh], %[rs], s62 offen offset:0 sc1\n"
      "s_cbranch_vccnz Lr9_%=\n"
      "Lo9_%=:\n"
      "s_waitcnt lgkmcnt(10)\n"
      "v_mfma_f32_16x16x32_bf16 v[68:71], v[8:11], v[120:123], v[68:71]\n"
      "ds_read_b128 v[8:11], v163 offset:10240\n"
      "v_mfma_f32_16x16x32_bf16 v[72:75], v[12:15], v[120:123], v[72:75]\n"
      "ds_read_b128 v[12:15], v163 offset:11264\n"
      "s_waitcnt vmcnt(4)\n"
      "v_max3_u32 v165, v124, v125, v126\n"
      "v_mfma_f32_16x16x32_bf16 v[76:79], v[16:19], v[120:123], v[76:79]\n"
      "ds_read_b128 v[16:19], v163 offset:12288\n"
      "v_max_u32 v165, v165, v127\n"
      "v_mfma_f32_16x16x32_bf16 v[80:83], v[20:23], v[120:123], v[80:83]\n"
      "ds_read_b128 v[20:23], v163 offset:13312\n"
      "v_cmp_eq_u32 vcc, -1, v165\n"
      "v_mfma_f32_16x16x32_bf16 v[84:87], v[24:27], v[120:123], v[84:87]\n"
      "ds_read_b128 v[24:27], v163 offset:14336\n"
      "s_add_u32 s62, %[ob], 36864\n"
      "buffer_load_dwordx4 v[120:123], %[vh], %[rs], s62 offen offset:0 sc1\n"
      "s_cbranch_vccnz Lr10_%=\n"
      "Lo10_%=:\n"
      "s_waitcnt lgkmcnt(10)\n"
      "v_mfma_f32_16x16x32_bf16 v[68:71], v[28:31], v[124:127], v[68:71]\n"
      "ds_read_b128 v[28:31], v163 offset:15360\n"
      "v_mfma_f32_16x16x32_bf16 v[72:75], v[32:35], v[124:127], v[72:75]\n"
      "ds_read_b128 v[32:35], v163 offset:16384\n"
      "s_waitcnt vmcnt(4)\n"
      "v_max3_u32 v165, v128, v129, v130\n"
      "v_mfma_f32_16x16x32_bf16 v[76:79], v[36:39], v[124:127], v[76:79]\n"
      "ds_read_b128 v[36:39], v163 offset:17408\n"
      "v_max_u32 v165, v165, v131\n"
      "v_mfma_f32_16x16x32_bf16 v[80:83], v[40:43], v[124:127], v[80:83]\n"
      "ds_read_b128 v[40:43], v163 offset:18432\n"
      "v_cmp_eq_u32 vcc, -1, v165\n"
      "v_mfma_f32_16x16x32_bf16 v[84:87], v[44:47], v[124:127], v[84:87]\n"
      "ds_read_b128 v[44:47], v163 offset:19456\n"
      "s_add_u32 s62, %[ob], 8192\n"
      "buffer_load_dwordx2 v[124:125], v164, %[rs], s62 offen offset:0 sc1\n"
      "buffer_load_dwordx2 v[126:127], v164, %[rs], s62 offen offset:256 sc1\n"
      "s_cbranch_vccnz Lr11_%=\n"
      "Lo11_%=:\n"
      "s_waitcnt lgkmcnt(10)\n"
      "v_mfma_f32_16x16x32_bf16 v[68:71], v[48:51], v[128:131], v[68:71]\n"
      "ds_read_b128 v[48:51], v163 offset:35840\n"
      "v_mfma_f32_16x16x32_bf16 v[72:75], v[52:55], v[128:131], v[72:75]\n"
      "ds_read_b128 v[52:55], v163 offset:36864\n"
      "s_waitcnt vmcnt(5)\n"
      "v_max3_u32 v165, v108, v109, v110\n"
      "v_mfma_f32_16x16x32_bf16 v[76:79], v[56:59], v[128:131], v[76:79]\n"
      "ds_read_b128 v[56:59], v163 offset:37888\n"
      "v_max_u32 v165, v165, v111\n"
      "v_mfma_f32_16x16x32_bf16 v[80:83], v[60:63], v[128:131], v[80:83]\n"
      "ds_read_b128 v[60:63], v163 offset:38912\n"
      "v_cmp_eq_u32 vcc, -1, v165\n"
      "v_mfma_f32_16x16x32_bf16 v[84:87], v[64:67], v[128:131], v[84:87]\n"
      "ds_read_b128 v[64:67], v163 offset:39936\n"
      "s_add_u32 s62, %[ob], 18432\n"
      "buffer_load_dwordx2 v[128:129], v164, %[rs], s62 offen offset:0 sc1\n"
      "buffer_load_dwordx2 v[130:131], v164, %[rs], s62 offen offset:256 sc1\n"
      "s_cbranch_vccnz Lr12_%=\n"
      "Lo12_%=:\n"
      "s_waitcnt lgkmcnt(10)\n"
      "v_mfma_f32_16x16x32_bf16 v[68:71], v[8:11], v[108:111], v[68:71]\n"
      "ds_read_b128 v[8:11], v163 offset:40960\n"
      "v_mfma_f32_16x16x32_bf16 v[72:75], v[12:15], v[108:111], v[72:75]\n"
      "ds_read_b128 v[12:15], v163 offset:41984\n"
      "s_waitcnt vmcnt(6)\n"
      "v_max3_u32 v165, v112, v113, v114\n"
      "v_mfma_f32_16x16x32_bf16 v[76:79], v[16:19], v[108:111], v[76:79]\n"
      "ds_read_b128 v[16:19], v163 offset:43008\n"
      "v_max_u32 v165, v165, v115\n"
      "v_mfma_f32_16x16x32_bf16 v[80:83], v[20:23], v[108:111], v[80:83]\n"
      "ds_read_b128 v[20:23], v163 offset:44032\n"
      "v_cmp_eq_u32 vcc, -1, v165\n"
      "v_mfma_f32_16x16x32_bf16 v[84:87], v[24:27], v[108:111], v[84:87]\n"
      "ds_read_b128 v[24:27], v163 offset:45056\n"
      "s_add_u32 s62, %[ob], 28672\n"
      "buffer_load_dwordx2 v[108:109], v164, %[rs], s62 offen offset:0 sc1\n"
      "buffer_load_dwordx2 v[110:111], v164, %[rs], s62 offen offset:256 sc1\n"
      "s_cbranch_vccnz Lr13_%=\n"
      "Lo13_%=:\n"
      "s_waitcnt lgkmcnt(10)\n"
      "v_mfma_f32_16x16x32_bf16 v[68:71], v[28:31], v[112:115], v[68:71]\n"
      "ds_read_b128 v[28:31], v162 offset:20480\n"
      "v_mfma_f32_16x16x32_bf16 v[72:75], v[32:35], v[112:115], v[72:75]\n"
      "ds_read_b128 v[32:35], v162 offset:21504\n"
      "s_waitcnt vmcnt(7)\n"
      "v_max3_u32 v165, v116, v117, v118\n"
      "v_mfma_f32_16x16x32_bf16 v[76:79], v[36:39], v[112:115], v[76:79]\n"
      "ds_read_b128 v[36:39], v162 offset:22528\n"
      "v_max_u32 v165, v165, v119\n"
      "v_mfma_f32_16x16x32_bf16 v[80:83], v[40:43], v[112:115], v[80:83]\n"
      "ds_read_b128 v[40:43], v162 offset:23552\n"
      "v_cmp_eq_u32 vcc, -1, v165\n"
      "v_mfma_f32_16x16x32_bf16 v[84:87], v[44:47], v[112:115], v[84:87]\n"
      "ds_read_b128 v[44:47], v162 offset:24576\n"
      "s_add_u32 s62, %[ob], 38912\n"
      "buffer_load_dwordx2 v[112:113], v164, %[rs], s62 offen offset:0 sc1\n"
      "buffer_load_dwordx2 v[114:115], v164, %[rs], s62 offen offset:256 sc1\n"
      "s_cbranch_vccnz Lr14_%=\n"
      "Lo14_%=:\n"
      "s_waitcnt lgkmcnt(10)\n"
      "v_mfma_f32_16x16x32_bf16 v[68:71], v[48:51], v[116:119], v[68:71]\n"
      "ds_read_b128 v[48:51], v162 offset:46080\n"
      "v_mfma_f32_16x16x32_bf16 v[72:75], v[52:55], v[116:119], v[72:75]\n"
      "ds_read_b128 v[52:55], v162 offset:47104\n"
      "s_waitcnt vmcnt(8)\n"
      "v_max3_u32 v165, v120, v121, v122\n"
      "v_mfma_f32_16x16x32_bf16 v[76:79], v[56:59], v[116:119], v[76:79]\n"
      "ds_read_b128 v[56:59], v162 offset:48128\n"
      "v_max_u32 v165, v165, v123\n"
      "v_mfma_f32_16x16x32_bf16 v[80:83], v[60:63], v[116:119], v[80:83]\n"
      "ds_read_b128 v[60:63], v162 offset:49152\n"
      "v_cmp_eq_u32 vcc, -1, v165\n"
      "v_mfma_f32_16x16x32_bf16 v[84:87], v[64:67], v[116:119], v[84:87]\n"
      "ds_read_b128 v[64:67], v162 offset:50176\n"
      "s_cbranch_vccnz Lr15_%=\n"
      "Lo15_%=:\n"
      "s_waitcnt lgkmcnt(10)\n"
      "v_mfma_f32_16x16x32_bf16 v[68:71], v[8:11], v[120:123], v[68:71]\n"
      "ds_read_b128 v[8:11], v163 offset:20480\n"
      "v_mfma_f32_16x16x32_bf16 v[72:75], v[12:15], v[120:123], v[72:75]\n"
      "ds_read_b128 v[12:15], v163 offset:21504\n"
      "s_waitcnt vmcnt(6)\n"
      "v_max3_u32 v165, v124, v125, v126\n"
      "v_mfma_f32_16x16x32_bf16 v[76:79], v[16:19], v[120:123], v[76:79]\n"
      "ds_read_b128 v[16:19], v163 offset:22528\n"
      "v_max_u32 v165, v165, v127\n"
      "v_mfma_f32_16x16x32_bf16 v[80:83], v[20:23], v[120:123], v[80:83]\n"
      "ds_read_b128 v[20:23], v163 offset:23552\n"
      "v_cmp_eq_u32 vcc, -1, v165\n"
      "v_mfma_f32_16x16x32_bf16 v[84:87], v[24:27], v[120:123], v[84:87]\n"
      "ds_read_b128 v[24:27], v163 offset:24576\n"
      "s_cbranch_vccnz Lr16_%=\n"
      "Lo16_%=:\n"
      "s_waitcnt lgkmcnt(10)\n"
      "v_mfma_f32_16x16x32_bf16 v[68:71], v[28:31], v[124:127], v[68:71]\n"
      "ds_read_b128 v[28:31], v163 offset:46080\n"
      "v_mfma_f32_16x16x32_bf16 v[72:75], v[32:35], v[124:127], v[72:75]\n"
      "ds_read_b128 v[32:35], v163 offset:47104\n"
      "s_waitcnt vmcnt(4)\n"
      "v_max3_u32 v165, v128, v129, v130\n"
      "v_mfma_f32_16x16x32_bf16 v[76:79], v[36:39], v[124:127], v[76:79]\n"
      "ds_read_b128 v[36:39], v163 offset:48128\n"
      "v_max_u32 v165, v165, v131\n"
      "v_mfma_f32_16x16x32_bf16 v[80:83], v[40:43], v[124:127], v[80:83]\n"
      "ds_read_b128 v[40:43], v163 offset:49152\n"
      "v_cmp_eq_u32 vcc, -1, v165\n"
      "v_mfma_f32_16x16x32_bf16 v[84:87], v[44:47], v[124:127], v[84:87]\n"
      "ds_read_b128 v[44:47], v163 offset:50176\n"
      "s_cbranch_vccnz Lr17_%=\n"
      "Lo17_%=:\n"
      "s_waitcnt lgkmcnt(10)\n"
      "v_mfma_f32_16x16x32_bf16 v[68:71], v[48:51], v[128:131], v[68:71]\n"
      "v_mfma_f32_16x16x32_bf16 v[72:75], v[52:55], v[128:131], v[72:75]\n"
      "s_waitcnt vmcnt(2)\n"
      "v_max3_u32 v165, v108, v109, v110\n"
      "v_mfma_f32_16x16x32_bf16 v[76:79], v[56:59], v[128:131], v[76:79]\n"
      "v_max_u32 v165, v165, v111\n"
      "v_mfma_f32_16x16x32_bf16 v[80:83], v[60:63], v[128:131], v[80:83]\n"
      "v_cmp_eq_u32 vcc, -1, v165\n"
      "v_mfma_f32_16x16x32_bf16 v[84:87], v[64:67], v[128:131], v[84:87]\n"
      "s_cbranch_vccnz Lr18_%=\n"
      "Lo18_%=:\n"
      "s_waitcnt lgkmcnt(5)\n"
      "v_mfma_f32_16x16x32_bf16 v[68:71], v[8:11], v[108:111], v[68:71]\n"
      "v_mfma_f32_16x16x32_bf16 v[72:75], v[12:15], v[108:111], v[72:75]\n"
      "s_waitcnt vmcnt(0)\n"
      "v_max3_u32 v165, v112, v113, v114\n"
      "v_mfma_f32_16x16x32_bf16 v[76:79], v[16:19], v[108:111], v[76:79]\n"
      "v_max_u32 v165, v165, v115\n"
      "v_mfma_f32_16x16x32_bf16 v[80:83], v[20:23], v[108:111], v[80:83]\n"
      "v_cmp_eq_u32 vcc, -1, v165\n"
      "v_mfma_f32_16x16x32_bf16 v[84:87], v[24:27], v[108:111], v[84:87]\n"
      "s_cbranch_vccnz Lr19_%=\n"
      "Lo19_%=:\n"
      "s_waitcnt lgkmcnt(0)\n"
      "v_mfma_f32_16x16x32_bf16 v[68:71], v[28:31], v[112:115], v[68:71]\n"
      "v_mfma_f32_16x16x32_bf16 v[72:75], v[32:35], v[112:115], v[72:75]\n"
      "v_mfma_f32_16x16x32_bf16 v[76:79], v[36:39], v[112:115], v[76:79]\n"
      "v_mfma_f32_16x16x32_bf16 v[80:83], v[40:43], v[112:115], v[80:83]\n"
      "v_mfma_f32_16x16x32_bf16 v[84:87], v[44:47], v[112:115], v[84:87]\n"
      "s_nop 3\n"
      "v_fmac_f32 v98, v68, v140\n"
      "v_fmac_f32 v88, v69, v141\n"
      "v_fmac_f32 v99, v70, v142\n"
      "v_fmac_f32 v89, v71, v143\n"
      "v_cvt_pk_bf16_f32 v88, v98, v88\n"
      "v_cvt_pk_bf16_f32 v89, v99, v89\n"
      "v_fmac_f32 v100, v72, v144\n"
      "v_fmac_f32 v90, v73, v145\n"
      "v_fmac_f32 v101, v74, v146\n"
      "v_fmac_f32 v91, v75, v147\n"
      "v_cvt_pk_bf16_f32 v90, v100, v90\n"
      "v_cvt_pk_bf16_f32 v91, v101, v91\n"
      "global_store_dwordx4 v160, v[88:91], %[op]\n"
      "v_fmac_f32 v102, v76, v148\n"
      "v_fmac_f32 v92, v77, v149\n"
      "v_fmac_f32 v103, v78, v150\n"
      "v_fmac_f32 v93, v79, v151\n"
      "v_cvt_pk_bf16_f32 v92, v102, v92\n"
      "v_cvt_pk_bf16_f32 v93, v103, v93\n"
      "v_fmac_f32 v104, v80, v152\n"
      "v_fmac_f32 v94, v81, v153\n"
      "v_fmac_f32 v105, v82, v154\n"
      "v_fmac_f32 v95, v83, v155\n"
      "v_cvt_pk_bf16_f32 v94, v104, v94\n"
      "v_cvt_pk_bf16_f32 v95, v105, v95\n"
      "global_store_dwordx4 v160, v[92:95], %[op] offset:64\n"
      "v_fmac_f32 v106, v84, v156\n"
      "v_fmac_f32 v96, v85, v157\n"
      "v_fmac_f32 v107, v86, v158\n"
      "v_fmac_f32 v97, v87, v159\n"
      "v_cvt_pk_bf16_f32 v96, v106, v96\n"
      "v_cvt_pk_bf16_f32 v97, v107, v97\n"
      "global_store_dwordx2 v161, v[96:97], %[op] offset:128\n"
      "s_branch Le%=\n"
      "Lr0_%=:\n"
      "s_cmp_eq_u32 s67, 0\n"
      "s_cbranch_scc1 Lo0_%=\n"
      "s_sub_u32 s67, s67, 1\n"
      "s_sleep 4\n"
      "s_add_u32 s62, %[ob], 0\n"
      "buffer_load_dwordx4 v[108:111], %[vh], %[rs], s62 offen offset:0 sc1\n"
      "s_add_u32 s62, %[ob], 2048\n"
      "buffer_load_dwordx4 v[112:115], %[vh], %[rs], s62 offen offset:0 sc1\n"
      "s_add_u32 s62, %[ob], 10240\n"
      "buffer_load_dwordx4 v[116:119], %[vh], %[rs], s62 offen offset:0 sc1\n"
      "s_add_u32 s62, %[ob], 12288\n"
      "buffer_load_dwordx4 v[120:123], %[vh], %[rs], s62 offen offset:0 sc1\n"
      "s_add_u32 s62, %[ob], 20480\n"
      "buffer_load_dwordx4 v[124:127], %[vh], %[rs], s62 offen offset:0 sc1\n"
      "s_add_u32 s62, %[ob], 22528\n"
      "buffer_load_dwordx4 v[128:131], %[vh], %[rs], s62 offen offset:0 sc1\n"
      "s_waitcnt vmcnt(5)\n"
      "v_max3_u32 v165, v108, v109, v110\n"
      "v_max_u32 v165, v165, v111\n"
      "v_cmp_eq_u32 vcc, -1, v165\n"
      "s_cbranch_vccnz Lr0_%=\n"
      "s_branch Lo0_%=\n"
      "Lr1_%=:\n"
      "s_cmp_eq_u32 s67, 0\n"
      "s_cbranch_scc1 Lo1_%=\n"
      "s_sub_u32 s67, s67, 1\n"
      "s_sleep 4\n"
      "s_add_u32 s62, %[ob], 2048\n"
      "buffer_load_dwordx4 v[112:115], %[vh], %[rs], s62 offen offset:0 sc1\n"
      "s_add_u32 s62, %[ob], 10240\n"
      "buffer_load_dwordx4 v[116:119], %[vh], %[rs], s62 offen offset:0 sc1\n"
      "s_add_u32 s62, %[ob], 12288\n"
      "buffer_load_dwordx4 v[120:123], %[vh], %[rs], s62 offen offset:0 sc1\n"
      "s_add_u32 s62, %[ob], 20480\n"
      "buffer_load_dwordx4 v[124:127], %[vh], %[rs], s62 offen offset:0 sc1\n"
      "s_add_u32 s62, %[ob], 22528\n"
      "buffer_load_dwordx4 v[128:131], %[vh], %[rs], s62 offen offset:0 sc1\n"
      "s_add_u32 s62, %[ob], 30720\n"
      "buffer_load_dwordx4 v[108:111], %[vh], %[rs], s62 offen offset:0 sc1\n"
      "s_waitcnt vmcnt(5)\n"
      "v_max3_u32 v165, v112, v113, v114\n"
      "v_max_u32 v165, v165, v115\n"
      "v_cmp_eq_u32 vcc, -1, v165\n"
      "s_cbranch_vccnz Lr1_%=\n"
      "s_branch Lo1_%=\n"
      "Lr2_%=:\n"
      "s_cmp_eq_u32 s67, 0\n"
      "s_cbranch_scc1 Lo2_%=\n"
      "s_sub_u32 s67, s67, 1\n"
      "s_sleep 4\n"
      "s_add_u32 s62, %[ob], 10240\n"
      "buffer_load_dwordx4 v[116:119], %[vh], %[rs], s62 offen offset:0 sc1\n"
      "s_add_u32 s62, %[ob], 12288\n"
      "buffer_load_dwordx4 v[120:123], %[vh], %[rs], s62 offen offset:0 sc1\n"
      "s_add_u32 s62, %[ob], 20480\n"
      "buffer_load_dwordx4 v[124:127], %[vh], %[rs], s62 offen offset:0 sc1\n"
      "s_add_u32 s62, %[ob], 22528\n"
      "buffer_load_dwordx4 v[128:131], %[vh], %[rs], s62 offen offset:0 sc1\n"
      "s_add_u32 s62, %[ob], 30720\n"
      "buffer_load_dwordx4 v[108:111], %[vh], %[rs], s62 offen offset:0 sc1\n"
      "s_add_u32 s62, %[ob], 32768\n"
      "buffer_load_dwordx4 v[112:115], %[vh], %[rs], s62 offen offset:0 sc1\n"
      "s_waitcnt vmcnt(5)\n"
      "v_max3_u32 v165, v116, v117, v118\n"
      "v_max_u32 v165, v165, v119\n"
      "v_cmp_eq_u32 vcc, -1, v165\n"
      "s_cbranch_vccnz Lr2_%=\n"
      "s_branch Lo2_%=\n"
      "Lr3_%=:\n"
      "s_cmp_eq_u32 s67, 0\n"
      "s_cbranch_scc1 Lo3_%=\n"
      "s_sub_u32 s67, s67, 1\n"
      "s_sleep 4\n"
      "s_add_u32 s62, %[ob], 12288\n"
      "buffer_load_dwordx4 v[120:123], %[vh], %[rs], s62 offen offset:0 sc1\n"
      "s_add_u32 s62, %[ob], 20480\n"
      "buffer_load_dwordx4 v[124:127], %[vh], %[rs], s62 offen offset:0 sc1\n"
      "s_add_u32 s62, %[ob], 22528\n"
      "buffer_load_dwordx4 v[128:131], %[vh], %[rs], s62 offen offset:0 sc1\n"
      "s_add_u32 s62, %[ob], 30720\n"
      "buffer_load_dwordx4 v[108:111], %[vh], %[rs], s62 offen offset:0 sc1\n"
      "s_add_u32 s62, %[ob], 32768\n"
      "buffer_load_dwordx4 v[112:115], %[vh], %[rs], s62 offen offset:0 sc1\n"
      "s_add_u32 s62, %[ob], 4096\n"
      "buffer_load_dwordx4 v[116:119], %[vh], %[rs], s62 offen offset:0 sc1\n"
      "s_waitcnt vmcnt(5)\n"
      "v_max3_u32 v165, v120, v121, v122\n"
      "v_max_u32 v165, v165, v123\n"
      "v_cmp_eq_u32 vcc, -1, v165\n"
      "s_cbranch_vccnz Lr3_%=\n"
      "s_branch Lo3_%=\n"
      "Lr4_%=:\n"
      "s_cmp_eq_u32 s67, 0\n"
      "s_cbranch_scc1 Lo4_%=\n"
      "s_sub_u32 s67, s67, 1\n"
      "s_sleep 4\n"
      "s_add_u32 s62, %[ob], 20480\n"
      "buffer_load_dwordx4 v[124:127], %[vh], %[rs], s62 offen offset:0 sc1\n"
      "s_add_u32 s62, %[ob], 22528\n"
      "buffer_load_dwordx4 v[128:131], %[vh], %[rs], s62 offen offset:0 sc1\n"
      "s_add_u32 s62, %[ob], 30720\n"
      "buffer_load_dwordx4 v[108:111], %[vh], %[rs], s62 offen offset:0 sc1\n"
      "s_add_u32 s62, %[ob], 32768\n"
      "buffer_load_dwordx4 v[112:115], %[vh], %[rs], s62 offen offset:0 sc1\n"
      "s_add_u32 s62, %[ob], 4096\n"
      "buffer_load_dwordx4 v[116:119], %[vh], %[rs], s62 offen offset:0 sc1\n"
      "s_add_u32 s62, %[ob], 6144\n"
      "buffer_load_dwordx4 v[120:123], %[vh], %[rs], s62 offen offset:0 sc1\n"
      "s_waitcnt vmcnt(5)\n"
      "v_max3_u32 v165, v124, v125, v126\n"
      "v_max_u32 v165, v165, v127\n"
      "v_cmp_eq_u32 vcc, -1, v165\n"
      "s_cbranch_vccnz Lr4_%=\n"
      "s_branch Lo4_%=\n"
      "Lr5_%=:\n"
      "s_cmp_eq_u32 s67, 0\n"
      "s_cbranch_scc1 Lo5_%=\n"
      "s_sub_u32 s67, s67, 1\n"
      "s_sleep 4\n"
      "s_add_u32 s62, %[ob], 22528\n"
      "buffer_load_dwordx4 v[128:131], %[vh], %[rs], s62 offen offset:0 sc1\n"
      "s_add_u32 s62, %[ob], 30720\n"
      "buffer_load_dwordx4 v[108:111], %[vh], %[rs], s62 offen offset:0 sc1\n"
      "s_add_u32 s62, %[ob], 32768\n"
      "buffer_load_dwordx4 v[112:115], %[vh], %[rs], s62 offen offset:0 sc1\n"
      "s_add_u32 s62, %[ob], 4096\n"
      "buffer_load_dwordx4 v[116:119], %[vh], %[rs], s62 offen offset:0 sc1\n"
      "s_add_u32 s62, %[ob], 6144\n"
      "buffer_load_dwordx4 v[120:123], %[vh], %[rs], s62 offen offset:0 sc1\n"
      "s_add_u32 s62, %[ob], 14336\n"
      "buffer_load_dwordx4 v[124:127], %[vh], %[rs], s62 offen offset:0 sc1\n"
      "s_waitcnt vmcnt(5)\n"
      "v_max3_u32 v165, v128, v129, v130\n"
      "v_max_u32 v165, v165, v131\n"
      "v_cmp_eq_u32 vcc, -1, v165\n"
      "s_cbranch_vccnz Lr5_%=\n"
      "s_branch Lo5_%=\n"
      "Lr6_%=:\n"
      "s_cmp_eq_u32 s67, 0\n"
      "s_cbranch_scc1 Lo6_%=\n"
      "s_sub_u32 s67, s67, 1\n"
      "s_sleep 4\n"
      "s_add_u32 s62, %[ob], 30720\n"
      "buffer_load_dwordx4 v[108:111], %[vh], %[rs], s62 offen offset:0 sc1\n"
      "s_add_u32 s62, %[ob], 32768\n"
      "buffer_load_dwordx4 v[112:115], %[vh], %[rs], s62 offen offset:0 sc1\n"
      "s_add_u32 s62, %[ob], 4096\n"
      "buffer_load_dwordx4 v[116:119], %[vh], %[rs], s62 offen offset:0 sc1\n"
      "s_add_u32 s62, %[ob], 6144\n"
      "buffer_load_dwordx4 v[120:123], %[vh], %[rs], s62 offen offset:0 sc1\n"
      "s_add_u32 s62, %[ob], 14336\n"
      "buffer_load_dwordx4 v[124:127], %[vh], %[rs], s62 offen offset:0 sc1\n"
      "s_add_u32 s62, %[ob], 16384\n"
      "buffer_load_dwordx4 v[128:131], %[vh], %[rs], s62 offen offset:0 sc1\n"
      "s_waitcnt vmcnt(5)\n"
      "v_max3_u32 v165, v108, v109, v110\n"
      "v_max_u32 v165, v165, v111\n"
      "v_cmp_eq_u32 vcc, -1, v165\n"
      "s_cbranch_vccnz Lr6_%=\n"
      "s_branch Lo6_%=\n"
      "Lr7_%=:\n"
      "s_cmp_eq_u32 s67, 0\n"
      "s_cbranch_scc1 Lo7_%=\n"
      "s_sub_u32 s67, s67, 1\n"
      "s_sleep 4\n"
      "s_add_u32 s62, %[ob], 32768\n"
      "buffer_load_dwordx4 v[112:115], %[vh], %[rs], s62 offen offset:0 sc1\n"
      "s_add_u32 s62, %[ob], 4096\n"
      "buffer_load_dwordx4 v[116:119], %[vh], %[rs], s62 offen offset:0 sc1\n"
      "s_add_u32 s62, %[ob], 6144\n"
      "buffer_load_dwordx4 v[120:123], %[vh], %[rs], s62 offen offset:0 sc1\n"
      "s_add_u32 s62, %[ob], 14336\n"
      "buffer_load_dwordx4 v[124:127], %[vh], %[rs], s62 offen offset:0 sc1\n"
      "s_add_u32 s62, %[ob], 16384\n"
      "buffer_load_dwordx4 v[128:131], %[vh], %[rs], s62 offen offset:0 sc1\n"
      "s_add_u32 s62, %[ob], 24576\n"
      "buffer_load_dwordx4 v[108:111], %[vh], %[rs], s62 offen offset:0 sc1\n"
      "s_waitcnt vmcnt(5)\n"
      "v_max3_u32 v165, v112, v113, v114\n"
      "v_max_u32 v165, v165, v115\n"
      "v_cmp_eq_u32 vcc, -1, v165\n"
      "s_cbranch_vccnz Lr7_%=\n"
      "s_branch Lo7_%=\n"
      "Lr8_%=:\n"
      "s_cmp_eq_u32 s67, 0\n"
      "s_cbranch_scc1 Lo8_%=\n"
      "s_sub_u32 s67, s67, 1\n"
      "s_sleep 4\n"
      "s_add_u32 s62, %[ob], 4096\n"
      "buffer_load_dwordx4 v[116:119], %[vh], %[rs], s62 offen offset:0 sc1\n"
      "s_add_u32 s62, %[ob], 6144\n"
      "buffer_load_dwordx4 v[120:123], %[vh], %[rs], s62 offen offset:0 sc1\n"
      "s_add_u32 s62, %[ob], 14336\n"
      "buffer_load_dwordx4 v[124:127], %[vh], %[rs], s62 offen offset:0 sc1\n"
      "s_add_u32 s62, %[ob], 16384\n"
      "buffer_load_dwordx4 v[128:131], %[vh], %[rs], s62 offen offset:0 sc1\n"
      "s_add_u32 s62, %[ob], 24576\n"
      "buffer_load_dwordx4 v[108:111], %[vh], %[rs], s62 offen offset:0 sc1\n"
      "s_add_u32 s62, %[ob], 26624\n"
      "buffer_load_dwordx4 v[112:115], %[vh], %[rs], s62 offen offset:0 sc1\n"
      "s_waitcnt vmcnt(5)\n"
      "v_max3_u32 v165, v116, v117, v118\n"
      "v_max_u32 v165, v165, v119\n"
      "v_cmp_eq_u32 vcc, -1, v165\n"
      "s_cbranch_vccnz Lr8_%=\n"
      "s_branch Lo8_%=\n"
      "Lr9_%=:\n"
      "s_cmp_eq_u32 s67, 0\n"
      "s_cbranch_scc1 Lo9_%=\n"
      "s_sub_u32 s67, s67, 1\n"
      "s_sleep 4\n"
      "s_add_u32 s62, %[ob], 6144\n"
      "buffer_load_dwordx4 v[120:123], %[vh], %[rs], s62 offen offset:0 sc1\n"
      "s_add_u32 s62, %[ob], 14336\n"
      "buffer_load_dwordx4 v[124:127], %[vh], %[rs], s62 offen offset:0 sc1\n"
      "s_add_u32 s62, %[ob], 16384\n"
      "buffer_load_dwordx4 v[128:131], %[vh], %[rs], s62 offen offset:0 sc1\n"
      "s_add_u32 s62, %[ob], 24576\n"
      "buffer_load_dwordx4 v[108:111], %[vh], %[rs], s62 offen offset:0 sc1\n"
      "s_add_u32 s62, %[ob], 26624\n"
      "buffer_load_dwordx4 v[112:115], %[vh], %[rs], s62 offen offset:0 sc1\n"
      "s_add_u32 s62, %[ob], 34816\n"
      "buffer_load_dwordx4 v[116:119], %[vh], %[rs], s62 offen offset:0 sc1\n"
      "s_waitcnt vmcnt(5)\n"
      "v_max3_u32 v165, v120, v121, v122\n"
      "v_max_u32 v165, v165, v123\n"
      "v_cmp_eq_u32 vcc, -1, v165\n"
      "s_cbranch_vccnz Lr9_%=\n"
      "s_branch Lo9_%=\n"
      "Lr10_%=:\n"
      "s_cmp_eq_u32 s67, 0\n"
      "s_cbranch_scc1 Lo10_%=\n"
      "s_sub_u32 s67, s67, 1\n"
      "s_sleep 4\n"
      "s_add_u32 s62, %[ob], 14336\n"
      "buffer_load_dwordx4 v[124:127], %[vh], %[rs], s62 offen offset:0 sc1\n"
      "s_add_u32 s62, %[ob], 16384\n"
      "buffer_load_dwordx4 v[128:131], %[vh], %[rs], s62 offen offset:0 sc1\n"
      "s_add_u32 s62, %[ob], 24576\n"
      "buffer_load_dwordx4 v[108:111], %[vh], %[rs], s62 offen offset:0 sc1\n"
      "s_add_u32 s62, %[ob], 26624\n"
      "buffer_load_dwordx4 v[112:115], %[vh], %[rs], s62 offen offset:0 sc1\n"
      "s_add_u32 s62, %[ob], 34816\n"
      "buffer_load_dwordx4 v[116:119], %[vh], %[rs], s62 offen offset:0 sc1\n"
      "s_add_u32 s62, %[ob], 36864\n"
      "buffer_load_dwordx4 v[120:123], %[vh], %[rs], s62 offen offset:0 sc1\n"
      "s_waitcnt vmcnt(5)\n"
      "v_max3_u32 v165, v124, v125, v126\n"
      "v_max_u32 v165, v165, v127\n"
      "v_cmp_eq_u32 vcc, -1, v165\n"
      "s_cbranch_vccnz Lr10_%=\n"
      "s_branch Lo10_%=\n"
      "Lr11_%=:\n"
      "s_cmp_eq_u32 s67, 0\n"
      "s_cbranch_scc1 Lo11_%=\n"
      "s_sub_u32 s67, s67, 1\n"
      "s_sleep 4\n"
      "s_add_u32 s62, %[ob], 16384\n"
      "buffer_load_dwordx4 v[128:131], %[vh], %[rs], s62 offen offset:0 sc1\n"
      "s_add_u32 s62, %[ob], 24576\n"
      "buffer_load_dwordx4 v[108:111], %[vh], %[rs], s62 offen offset:0 sc1\n"
      "s_add_u32 s62, %[ob], 26624\n"
      "buffer_load_dwordx4 v[112:115], %[vh], %[rs], s62 offen offset:0 sc1\n"
      "s_add_u32 s62, %[ob], 34816\n"
      "buffer_load_dwordx4 v[116:119], %[vh], %[rs], s62 offen offset:0 sc1\n"
      "s_add_u32 s62, %[ob], 36864\n"
      "buffer_load_dwordx4 v[120:123], %[vh], %[rs], s62 offen offset:0 sc1\n"
      "s_add_u32 s62, %[ob], 8192\n"
      "buffer_load_dwordx2 v[124:125], v164, %[rs], s62 offen offset:0 sc1\n"
      "buffer_load_dwordx2 v[126:127], v164, %[rs], s62 offen offset:256 sc1\n"
      "s_waitcnt vmcnt(6)\n"
      "v_max3_u32 v165, v128, v129, v130\n"
      "v_max_u32 v165, v165, v131\n"
      "v_cmp_eq_u32 vcc, -1, v165\n"
      "s_cbranch_vccnz Lr11_%=\n"
      "s_branch Lo11_%=\n"
      "Lr12_%=:\n"
      "s_cmp_eq_u32 s67, 0\n"
      "s_cbranch_scc1 Lo12_%=\n"
      "s_sub_u32 s67, s67, 1\n"
      "s_sleep 4\n"
      "s_add_u32 s62, %[ob], 24576\n"
      "buffer_load_dwordx4 v[108:111], %[vh], %[rs], s62 offen offset:0 sc1\n"
      "s_add_u32 s62, %[ob], 26624\n"
      "buffer_load_dwordx4 v[112:115], %[vh], %[rs], s62 offen offset:0 sc1\n"
      "s_add_u32 s62, %[ob], 34816\n"
      "buffer_load_dwordx4 v[116:119], %[vh], %[rs], s62 offen offset:0 sc1\n"
      "s_add_u32 s62, %[ob], 36864\n"
      "buffer_load_dwordx4 v[120:123], %[vh], %[rs], s62 offen offset:0 sc1\n"
      "s_add_u32 s62, %[ob], 8192\n"
      "buffer_load_dwordx2 v[124:125], v164, %[rs], s62 offen offset:0 sc1\n"
      "buffer_load_dwordx2 v[126:127], v164, %[rs], s62 offen offset:256 sc1\n"
      "s_add_u32 s62, %[ob], 18432\n"
      "buffer_load_dwordx2 v[128:129], v164, %[rs], s62 offen offset:0 sc1\n"
      "buffer_load_dwordx2 v[130:131], v164, %[rs], s62 offen offset:256 sc1\n"
      "s_waitcnt vmcnt(7)\n"
      "v_max3_u32 v165, v108, v109, v110\n"
      "v_max_u32 v165, v165, v111\n"
      "v_cmp_eq_u32 vcc, -1, v165\n"
      "s_cbranch_vccnz Lr12_%=\n"
      "s_branch Lo12_%=\n"
      "Lr13_%=:\n"
      "s_cmp_eq_u32 s67, 0\n"
      "s_cbranch_scc1 Lo13_%=\n"
      "s_sub_u32 s67, s67, 1\n"
      "s_sleep 4\n"
      "s_add_u32 s62, %[ob], 26624\n"
      "buffer_load_dwordx4 v[112:115], %[vh], %[rs], s62 offen offset:0 sc1\n"
      "s_add_u32 s62, %[ob], 34816\n"
      "buffer_load_dwordx4 v[116:119], %[vh], %[rs], s62 offen offset:0 sc1\n"
      "s_add_u32 s62, %[ob], 36864\n"
      "buffer_load_dwordx4 v[120:123], %[vh], %[rs], s62 offen offset:0 sc1\n"
      "s_add_u32 s62, %[ob], 8192\n"
      "buffer_load_dwordx2 v[124:125], v164, %[rs], s62 offen offset:0 sc1\n"
      "buffer_load_dwordx2 v[126:127], v164, %[rs], s62 offen offset:256 sc1\n"
      "s_add_u32 s62, %[ob], 18432\n"
      "buffer_load_dwordx2 v[128:129], v164, %[rs], s62 offen offset:0 sc1\n"
      "buffer_load_dwordx2 v[130:131], v164, %[rs], s62 offen offset:256 sc1\n"
      "s_add_u32 s62, %[ob], 28672\n"
      "buffer_load_dwordx2 v[108:109], v164, %[rs], s62 offen offset:0 sc1\n"
      "buffer_load_dwordx2 v[110:111], v164, %[rs], s62 offen offset:256 sc1\n"
      "s_waitcnt vmcnt(8)\n"
      "v_max3_u32 v165, v112, v113, v114\n"
      "v_max_u32 v165, v165, v115\n"
      "v_cmp_eq_u32 vcc, -1, v165\n"
      "s_cbranch_vccnz Lr13_%=\n"
      "s_branch Lo13_%=\n"
      "Lr14_%=:\n"
      "s_cmp_eq_u32 s67, 0\n"
      "s_cbranch_scc1 Lo14_%=\n"
      "s_sub_u32 s67, s67, 1\n"
      "s_sleep 4\n"
      "s_add_u32 s62, %[ob], 34816\n"
      "buffer_load_dwordx4 v[116:119], %[vh], %[rs], s62 offen offset:0 sc1\n"
      "s_add_u32 s62, %[ob], 36864\n"
      "buffer_load_dwordx4 v[120:123], %[vh], %[rs], s62 offen offset:0 sc1\n"
      "s_add_u32 s62, %[ob], 8192\n"
      "buffer_load_dwordx2 v[124:125], v164, %[rs], s62 offen offset:0 sc1\n"
      "buffer_load_dwordx2 v[126:127], v164, %[rs], s62 offen offset:256 sc1\n"
      "s_add_u32 s62, %[ob], 18432\n"
      "buffer_load_dwordx2 v[128:129], v164, %[rs], s62 offen offset:0 sc1\n"
      "buffer_load_dwordx2 v[130:131], v164, %[rs], s62 offen offset:256 sc1\n"
      "s_add_u32 s62, %[ob], 28672\n"
      "buffer_load_dwordx2 v[108:109], v164, %[rs], s62 offen offset:0 sc1\n"
      "buffer_load_dwordx2 v[110:111], v164, %[rs], s62 offen offset:256 sc1\n"
      "s_add_u32 s62, %[ob], 38912\n"
      "buffer_load_dwordx2 v[112:113], v164, %[rs], s62 offen offset:0 sc1\n"
      "buffer_load_dwordx2 v[114:115], v164, %[rs], s62 offen offset:256 sc1\n"
      "s_waitcnt vmcnt(9)\n"
      "v_max3_u32 v165, v116, v117, v118\n"
      "v_max_u32 v165, v165, v119\n"
      "v_cmp_eq_u32 vcc, -1, v165\n"
      "s_cbranch_vccnz Lr14_%=\n"
      "s_branch Lo14_%=\n"
      "Lr15_%=:\n"
      "s_cmp_eq_u32 s67, 0\n"
      "s_cbranch_scc1 Lo15_%=\n"
      "s_sub_u32 s67, s67, 1\n"
      "s_sleep 4\n"
      "s_add_u32 s62, %[ob], 36864\n"
      "buffer_load_dwordx4 v[120:123], %[vh], %[rs], s62 offen offset:0 sc1\n"
      "s_add_u32 s62, %[ob], 8192\n"
      "buffer_load_dwordx2 v[124:125], v164, %[rs], s62 offen offset:0 sc1\n"
      "buffer_load_dwordx2 v[126:127], v164, %[rs], s62 offen offset:256 sc1\n"
      "s_add_u32 s62, %[ob], 18432\n"
      "buffer_load_dwordx2 v[128:129], v164, %[rs], s62 offen offset:0 sc1\n"
      "buffer_load_dwordx2 v[130:131], v164, %[rs], s62 offen offset:256 sc1\n"
      "s_add_u32 s62, %[ob], 28672\n"
      "buffer_load_dwordx2 v[108:109], v164, %[rs], s62 offen offset:0 sc1\n"
      "buffer_load_dwordx2 v[110:111], v164, %[rs], s62 offen offset:256 sc1\n"
      "s_add_u32 s62, %[ob], 38912\n"
      "buffer_load_dwordx2 v[112:113], v164, %[rs], s62 offen offset:0 sc1\n"
      "buffer_load_dwordx2 v[114:115], v164, %[rs], s62 offen offset:256 sc1\n"
      "s_waitcnt vmcnt(8)\n"
      "v_max3_u32 v165, v120, v121, v122\n"
      "v_max_u32 v165, v165, v123\n"
      "v_cmp_eq_u32 vcc, -1, v165\n"
      "s_cbranch_vccnz Lr15_%=\n"
      "s_branch Lo15_%=\n"
      "Lr16_%=:\n"
      "s_cmp_eq_u32 s67, 0\n"
      "s_cbranch_scc1 Lo16_%=\n"
      "s_sub_u32 s67, s67, 1\n"
      "s_sleep 4\n"
      "s_add_u32 s62, %[ob], 8192\n"
      "buffer_load_dwordx2 v[124:125], v164, %[rs], s62 offen offset:0 sc1\n"
      "buffer_load_dwordx2 v[126:127], v164, %[rs], s62 offen offset:256 sc1\n"
      "s_add_u32 s62, %[ob], 18432\n"
      "buffer_load_dwordx2 v[128:129], v164, %[rs], s62 offen offset:0 sc1\n"
      "buffer_load_dwordx2 v[130:131], v164, %[rs], s62 offen offset:256 sc1\n"
      "s_add_u32 s62, %[ob], 28672\n"
      "buffer_load_dwordx2 v[108:109], v164, %[rs], s62 offen offset:0 sc1\n"
      "buffer_load_dwordx2 v[110:111], v164, %[rs], s62 offen offset:256 sc1\n"
      "s_add_u32 s62, %[ob], 38912\n"
      "buffer_load_dwordx2 v[112:113], v164, %[rs], s62 offen offset:0 sc1\n"
      "buffer_load_dwordx2 v[114:115], v164, %[rs], s62 offen offset:256 sc1\n"
      "s_waitcnt vmcnt(6)\n"
      "v_max3_u32 v165, v124, v125, v126\n"
      "v_max_u32 v165, v165, v127\n"
      "v_cmp_eq_u32 vcc, -1, v165\n"
      "s_cbranch_vccnz Lr16_%=\n"
      "s_branch Lo16_%=\n"
      "Lr17_%=:\n"
      "s_cmp_eq_u32 s67, 0\n"
      "s_cbranch_scc1 Lo17_%=\n"
      "s_sub_u32 s67, s67, 1\n"
      "s_sleep 4\n"
      "s_add_u32 s62, %[ob], 18432\n"
      "buffer_load_dwordx2 v[128:129], v164, %[rs], s62 offen offset:0 sc1\n"
      "buffer_load_dwordx2 v[130:131], v164, %[rs], s62 offen offset:256 sc1\n"
      "s_add_u32 s62, %[ob], 28672\n"
      "buffer_load_dwordx2 v[108:109], v164, %[rs], s62 offen offset:0 sc1\n"
      "buffer_load_dwordx2 v[110:111], v164, %[rs], s62 offen offset:256 sc1\n"
      "s_add_u32 s62, %[ob], 38912\n"
      "buffer_load_dwordx2 v[112:113], v164, %[rs], s62 offen offset:0 sc1\n"
      "buffer_load_dwordx2 v[114:115], v164, %[rs], s62 offen offset:256 sc1\n"
      "s_waitcnt vmcnt(4)\n"
      "v_max3_u32 v165, v128, v129, v130\n"
      "v_max_u32 v165, v165, v131\n"
      "v_cmp_eq_u32 vcc, -1, v165\n"
      "s_cbranch_vccnz Lr17_%=\n"
      "s_branch Lo17_%=\n"
      "Lr18_%=:\n"
      "s_cmp_eq_u32 s67, 0\n"
      "s_cbranch_scc1 Lo18_%=\n"
      "s_sub_u32 s67, s67, 1\n"
      "s_sleep 4\n"
      "s_add_u32 s62, %[ob], 28672\n"
      "buffer_load_dwordx2 v[108:109], v164, %[rs], s62 offen offset:0 sc1\n"
      "buffer_load_dwordx2 v[110:111], v164, %[rs], s62 offen offset:256 sc1\n"
      "s_add_u32 s62, %[ob], 38912\n"
      "buffer_load_dwordx2 v[112:113], v164, %[rs], s62 offen offset:0 sc1\n"
      "buffer_load_dwordx2 v[114:115], v164, %[rs], s62 offen offset:256 sc1\n"
      "s_waitcnt vmcnt(2)\n"
      "v_max3_u32 v165, v108, v109, v110\n"
      "v_max_u32 v165, v165, v111\n"
      "v_cmp_eq_u32 vcc, -1, v165\n"
      "s_cbranch_vccnz Lr18_%=\n"
      "s_branch Lo18_%=\n"
      "Lr19_%=:\n"
      "s_cmp_eq_u32 s67, 0\n"
      "s_cbranch_scc1 Lo19_%=\n"
      "s_sub_u32 s67, s67, 1\n"
      "s_sleep 4\n"
      "s_add_u32 s62, %[ob], 38912\n"
      "buffer_load_dwordx2 v[112:113], v164, %[rs], s62 offen offset:0 sc1\n"
      "buffer_load_dwordx2 v[114:115], v164, %[rs], s62 offen offset:256 sc1\n"
      "s_waitcnt vmcnt(0)\n"
      "v_max3_u32 v165, v112, v113, v114\n"
      "v_max_u32 v165, v165, v115\n"
      "v_cmp_eq_u32 vcc, -1, v165\n"
      "s_cbranch_vccnz Lr19_%=\n"
      "s_branch Lo19_%=\n"
      "Le%=:\n"
      :
      : [l16] "v"(lane * 16), [vh] "v"((lane >> 4) * 512 + (w & 1) * 256 + (lane & 15) * 16),
        [ad] "v"(lbase + SY_AD + (lane & 7) * 4), [sgw] "v"(lbase + SY_G2 + 4 * w), [sgr] "v"(lbase + SY_G2 + 4 * p4),
        [rs] "s"(srd), [ha] "s"(hbase + (w >> 1) * 98304), [hb] "s"(hbase + ((w >> 1) ^ 1) * 98304), [wo] "s"(WS_W2F + (cg * 48 + 12 * w) * 5120),
        [rot] "s"(((bi + (bi >> 2)) & 1) * 6), [w] "s"(w), [lw] "s"(lbase + L_WOP + w * 1024), [fw] "s"(WS_WOF + cg * 102400 + w * 1024),
        [wb] "s"(lbase + L_WOP), [par] "s"(xcd & 1), [ob] "s"(WS_O + (2 * rt + (w >> 1)) * 40960), [ex] "s"(lbase + 96768),
        [xp] "s"(x + fo), [op] "s"(out + fo), [bp] "s"(b2 + 160 * cg), [ro] "s"(WS_ST + 32768 + (64 * rt + 16 * w) * 8 + 4)
      : "v8", "v9", "v10", "v11", "v12", "v13", "v14", "v15", "v16", "v17", "v18", "v19", "v20", "v21", "v22", "v23", "v24", "v25", "v26", "v27", "v28", "v29", "v30", "v31", "v32", "v33", "v34", "v35", "v36", "v37", "v38", "v39", "v40", "v41", "v42", "v43", "v44", "v45", "v46", "v47", "v48", "v49", "v50", "v51", "v52", "v53", "v54", "v55", "v56", "v57", "v58", "v59", "v60", "v61", "v62", "v63", "v64", "v65", "v66", "v67", "v68", "v69", "v70", "v71", "v72", "v73", "v74", "v75", "v76", "v77", "v78", "v79", "v80", "v81", "v82", "v83", "v84", "v85", "v86", "v87", "v88", "v89", "v90", "v91", "v92", "v93", "v94", "v95", "v96", "v97", "v98", "v99", "v100", "v101", "v102", "v103", "v104", "v105", "v106", "v107", "v108", "v109", "v110", "v111", "v112", "v113", "v114", "v115", "v116", "v117", "v118", "v119", "v120", "v121", "v122", "v123", "v124", "v125", "v126", "v127", "v128", "v129", "v130", "v131", "v132", "v133", "v134", "v135", "v136", "v137", "v138", "v139", "v140", "v141", "v142", "v143", "v144", "v145", "v146", "v147", "v148", "v149", "v150", "v151", "v152", "v153", "v154", "v155", "v156", "v157", "v158", "v159", "v160", "v161", "v162", "v163", "v164", "v165", "v166", "v167", "s60", "s61", "s62", "s63", "s64", "s65", "s66", "s67", "vcc", "scc", "memory");
  } else {
    LANEVARS
    const u32 srow = 128 * rb + 8 * cb + 2 * w + hh;
    const u32 xro = srow * 1280 + tok * 16;
    const u32 tq = 8 * qb + 2 * w + grp;
    const u32 ob = WS_O + tq * 40960 + (head >> 3) * 10240, om = ob + (head & 7) * 1024, orr = ob + 8192 + (head & 7) * 256;
    u32 lk, lr, lv, sr, sw, lq, lqb, lqr, ad, one;
    auto attn_addr = [&]() LAM {
      LANEVARS
      lk = lbase + grp * 1024 + lane * 16; lr = lbase + grp * 512 + tok * 16 + 8 * odd;
      lv = lbase + grp * 1280 + hh * 320 + tok * 16; one = tok == 20 ? 0x3f803f80u : 0u;
      sr = lbase + SY_RD + p4 * 4; sw = lbase + SY_AD + wave * 4; ad = lbase + SY_AD + (lane & 7) * 4;
      lq = lk + L_Q + QOFF(w); lqb = lbase + L_Q + QOFF(w) + (1 - grp) * 1024 + lane * 16;
      lqr = lbase + L_Q + QOFF(w) + 2048 + 512 * (hh ^ grp) + tok * 16 + 8 * odd;
      asm volatile("" : "+v"(lq), "+v"(lqb), "+v"(lqr), "+v"(lk), "+v"(lr), "+v"(lv), "+v"(sr), "+v"(sw), "+v"(ad), "+v"(one) : "s"(om), "s"(orr));
    };
    u64 tag;
    const u32 on = hh == ((lane >> 1) & 1) ? 0x3f803f80u : 0u, o0 = (lane & 4) ? 0u : on, o1 = (lane & 4) ? on : 0u;
    const u32 ix = (lane & 1) ? 0xeeeeeeeeu : 0x44444444u, hm = hh ? 0xffff0000u : 0u;
    if (grp == 0) {
      tag = *epoch + 1;
      if (w == 0) *(LAS u32*)(L + L_SY + lane * 4) = lane < 16 ? 0 : ~(u32)tag;
      const u32 mi = w >> 1, ni = w & 1, ty = qc / 5, b5 = qc - ty * 5;
      const bool vt = ty == 2;
      const u32 row = vt ? tau(tok) : tok, pp = vt ? p4 : (((p4 & 1) << 1) | (p4 >> 1));
      const u32 xa = lbase + 65536 + (64 * mi + row) * 128 + ((hh ^ ((row >> 1) & 7)) << 4);
      const u32 wa0 = lbase + L_W + (8 * hh + q4) * 256 + (((8 * ni + 2 * g16 + (pp >> 1)) ^ (q4 << 2)) << 4) + (pp & 1) * 8, wa1 = wa0 ^ 64;
      const u32 tfa = lbase + L_ST + (64 * mi + row) * 8 + 4, rsa = lbase + L_ST + 2048 + (64 * mi + (vt ? 8 * hh : tok)) * 8 + 4;
      const u32 vo = vt ? hh * 10240 + tok * 16 : lane * 16;
      const u32 so = ty * WS_KP + (4 * rb + 2 * mi) * 40960 + (vt ? 2048 * b5 + 1024 * ni : 8192 * b5 + 4096 * ni);
      STAMP(3);
#define G0_IN [xa] "v"(xa), [wa0] "v"(wa0), [wa1] "v"(wa1), [o0] "v"(o0), [o1] "v"(o1), [ix] "v"(ix)
#define G0_EP [hm] "v"(hm), [tfa] "v"(tfa), [rsa] "v"(rsa), [vo] "v"(vo), [rs] "s"(srd), [so] "s"(so)
      if (qc == 15) {
        CVT_GEOMETRY(512 + 2 * rb + (w & 1))
        bar(); bar(); bar();
        const u32 warm = *(GAS const u32*)(cvb + (tok + e8 * (tok >> 3) + e16 * (tok >> 4)) * 1280 + hh * 128);
#pragma unroll 1
        for (int i = 0; i < 8 + 1; ++i) bar();
        asm volatile("" :: "v"(warm));
        __builtin_amdgcn_s_sleep(48);
      } else {
        if (vt) {
          asm volatile(
      "v_mov_b32 v52, %[xa]\n"
      "v_xor_b32 v53, 32, %[xa]\n"
      "v_xor_b32 v54, 64, %[xa]\n"
      "v_xor_b32 v55, 96, %[xa]\n"
      "v_mov_b32 v50, %[wa0]\n"
      "v_mov_b32 v51, %[wa1]\n"
      "v_mov_b32 v56, %[o0]\n"
      "v_mov_b32 v57, %[o0]\n"
      "v_mov_b32 v58, %[o1]\n"
      "v_mov_b32 v59, %[o1]\n"
      "v_mov_b32 v60, %[ix]\n"
      "v_mov_b32 v160, 0\n"
      "v_mov_b32 v161, 0\n"
      "v_mov_b32 v162, 0\n"
      "v_mov_b32 v163, 0\n"
      "v_mov_b32 v45, 0\n"
      "v_mov_b32 v47, 0\n"
      "s_nop 1\n"
      "v_mfma_f32_32x32x16_bf16 v[64:79], v[160:163], v[160:163], 0\n"
      "v_mfma_f32_32x32x16_bf16 v[80:95], v[160:163], v[160:163], 0\n"
      "v_mfma_f32_32x32x16_bf16 v[96:111], v[160:163], v[160:163], 0\n"
      "v_mfma_f32_32x32x16_bf16 v[112:127], v[160:163], v[160:163], 0\n"
      "s_mov_b32 s60, 4\n"
      "s_mov_b32 s61, 0\n"
      "s_mov_b32 s62, 10\n"
      "s_waitcnt lgkmcnt(0)\n"
      "s_barrier\n"
      "ds_read_b128 v[128:131], v52 offset:0\n"
      "ds_read_b64_tr_b16 v[136:137], v50 offset:0\n"
      "ds_read_b64_tr_b16 v[138:139], v50 offset:1024\n"
      "ds_read_b128 v[132:135], v52 offset:4096\n"
      "ds_read_b64_tr_b16 v[140:141], v51 offset:0\n"
      "ds_read_b64_tr_b16 v[142:143], v51 offset:1024\n"
      "Lp%=:\n"
      "s_waitcnt lgkmcnt(3)\n"
      "v_mfma_f32_32x32x16_bf16 v[64:79], v[128:131], v[136:139], v[64:79]\n"
      "ds_read_b128 v[144:147], v53 offset:0\n"
      "ds_read_b64_tr_b16 v[152:153], v50 offset:4096\n"
      "ds_read_b64_tr_b16 v[154:155], v50 offset:5120\n"
      "s_waitcnt lgkmcnt(3)\n"
      "v_mfma_f32_32x32x16_bf16 v[80:95], v[132:135], v[136:139], v[80:95]\n"
      "ds_read_b128 v[148:151], v53 offset:4096\n"
      "ds_read_b64_tr_b16 v[156:157], v51 offset:4096\n"
      "ds_read_b64_tr_b16 v[158:159], v51 offset:5120\n"
      "v_mfma_f32_32x32x16_bf16 v[96:111], v[128:131], v[140:143], v[96:111]\n"
      "s_add_u32 s60, s60, 1\n"
      "s_cmp_eq_u32 s60, 6\n"
      "s_mov_b32 s63, 16384\n"
      "s_cmov_b32 s63, 4294885376\n"
      "v_mfma_f32_32x32x16_bf16 v[112:127], v[132:135], v[140:143], v[112:127]\n"
      "s_cmov_b32 s60, 0\n"
      "v_smfmac_f32_16x16x64_bf16 v[160:163], v[56:59], v[136:143], v60\n"
      "s_waitcnt lgkmcnt(3)\n"
      "v_mfma_f32_32x32x16_bf16 v[64:79], v[144:147], v[152:155], v[64:79]\n"
      "ds_read_b128 v[128:131], v54 offset:0\n"
      "ds_read_b64_tr_b16 v[136:137], v50 offset:8192\n"
      "ds_read_b64_tr_b16 v[138:139], v50 offset:9216\n"
      "s_waitcnt lgkmcnt(3)\n"
      "v_mfma_f32_32x32x16_bf16 v[80:95], v[148:151], v[152:155], v[80:95]\n"
      "ds_read_b128 v[132:135], v54 offset:4096\n"
      "ds_read_b64_tr_b16 v[140:141], v51 offset:8192\n"
      "ds_read_b64_tr_b16 v[142:143], v51 offset:9216\n"
      "v_mfma_f32_32x32x16_bf16 v[96:111], v[144:147], v[156:159], v[96:111]\n"
      "s_add_u32 s61, s61, 1\n"
      "s_cmp_eq_u32 s61, 3\n"
      "s_mov_b32 s64, 16384\n"
      "s_cmov_b32 s64, 4294934528\n"
      "v_mfma_f32_32x32x16_bf16 v[112:127], v[148:151], v[156:159], v[112:127]\n"
      "s_cmov_b32 s61, 0\n"
      "v_smfmac_f32_16x16x64_bf16 v[160:163], v[56:59], v[152:159], v60\n"
      "s_waitcnt lgkmcnt(3)\n"
      "v_mfma_f32_32x32x16_bf16 v[64:79], v[128:131], v[136:139], v[64:79]\n"
      "ds_read_b128 v[144:147], v55 offset:0\n"
      "ds_read_b64_tr_b16 v[152:153], v50 offset:12288\n"
      "ds_read_b64_tr_b16 v[154:155], v50 offset:13312\n"
      "s_waitcnt lgkmcnt(3)\n"
      "v_mfma_f32_32x32x16_bf16 v[80:95], v[132:135], v[136:139], v[80:95]\n"
      "ds_read_b128 v[148:151], v55 offset:4096\n"
      "ds_read_b64_tr_b16 v[156:157], v51 offset:12288\n"
      "ds_read_b64_tr_b16 v[158:159], v51 offset:13312\n"
      "v_mfma_f32_32x32x16_bf16 v[96:111], v[128:131], v[140:143], v[96:111]\n"
      "v_add_u32 v52, s63, v52\n"
      "v_add_u32 v53, s63, v53\n"
      "v_add_u32 v54, s63, v54\n"
      "v_add_u32 v55, s63, v55\n"
      "v_mfma_f32_32x32x16_bf16 v[112:127], v[132:135], v[140:143], v[112:127]\n"
      "v_add_u32 v50, s64, v50\n"
      "v_add_u32 v51, s64, v51\n"
      "s_sub_u32 s62, s62, 1\n"
      "s_cmp_eq_u32 s62, 0\n"
      "v_smfmac_f32_16x16x64_bf16 v[160:163], v[56:59], v[136:143], v60\n"
      "s_waitcnt lgkmcnt(0)\n"
      "v_mfma_f32_32x32x16_bf16 v[64:79], v[144:147], v[152:155], v[64:79]\n"
      "s_barrier\n"
      "s_cbranch_scc1 Lx%=\n"
      "ds_read_b128 v[128:131], v52 offset:0\n"
      "ds_read_b64_tr_b16 v[136:137], v50 offset:0\n"
      "ds_read_b64_tr_b16 v[138:139], v50 offset:1024\n"
      "v_mfma_f32_32x32x16_bf16 v[80:95], v[148:151], v[152:155], v[80:95]\n"
      "ds_read_b128 v[132:135], v52 offset:4096\n"
      "ds_read_b64_tr_b16 v[140:141], v51 offset:0\n"
      "ds_read_b64_tr_b16 v[142:143], v51 offset:1024\n"
      "v_mfma_f32_32x32x16_bf16 v[96:111], v[144:147], v[156:159], v[96:111]\n"
      "v_mfma_f32_32x32x16_bf16 v[112:127], v[148:151], v[156:159], v[112:127]\n"
      "v_smfmac_f32_16x16x64_bf16 v[160:163], v[56:59], v[152:159], v60\n"
      "s_branch Lp%=\n"
      "Lx%=:\n"
      "s_setprio 2\n"
      "v_smfmac_f32_16x16x64_bf16 v[160:163], v[56:59], v[152:159], v60\n"
      "s_waitcnt vmcnt(0)\n"
      "ds_read2st64_b32 v[164:165], %[tfa] offset0:0 offset1:4\n"
      "ds_read2st64_b32 v[166:167], %[tfa] offset0:1 offset1:5\n"
      "ds_read2_b32 v[128:129], %[rsa] offset0:0 offset1:2\n"
      "ds_read2_b32 v[130:131], %[rsa] offset0:4 offset1:6\n"
      "ds_read2_b32 v[132:133], %[rsa] offset0:8 offset1:10\n"
      "ds_read2_b32 v[134:135], %[rsa] offset0:12 offset1:14\n"
      "ds_read2_b32 v[136:137], %[rsa] offset0:32 offset1:34\n"
      "ds_read2_b32 v[138:139], %[rsa] offset0:36 offset1:38\n"
      "ds_read2_b32 v[140:141], %[rsa] offset0:40 offset1:42\n"
      "ds_read2_b32 v[142:143], %[rsa] offset0:44 offset1:46\n"
      "v_mfma_f32_32x32x16_bf16 v[80:95], v[148:151], v[152:155], v[80:95]\n"
      "s_nop 3\n"
      "v_add_f32 v160, v160, v161\n"
      "v_add_f32 v162, v162, v163\n"
      "v_cvt_pk_bf16_f32 v161, v160, v160\n"
      "v_cvt_pk_bf16_f32 v163, v162, v162\n"
      "v_mfma_f32_32x32x16_bf16 v[96:111], v[144:147], v[156:159], v[96:111]\n"
      "v_and_b32 v161, %[hm], v161\n"
      "v_and_b32 v163, %[hm], v163\n"
      "v_sub_f32 v160, v160, v161\n"
      "v_sub_f32 v162, v162, v163\n"
      "v_cvt_pk_bf16_f32 v44, v160, v160\n"
      "v_cvt_pk_bf16_f32 v46, v162, v162\n"
      "s_waitcnt lgkmcnt(0)\n"
      "v_mfma_f32_32x32x16_bf16 v[112:127], v[148:151], v[156:159], v[112:127]\n"
      "ds_read2_b32 v[144:145], %[rsa] offset0:64 offset1:66\n"
      "ds_read2_b32 v[146:147], %[rsa] offset0:68 offset1:70\n"
      "ds_read2_b32 v[148:149], %[rsa] offset0:72 offset1:74\n"
      "ds_read2_b32 v[150:151], %[rsa] offset0:76 offset1:78\n"
      "ds_read2_b32 v[152:153], %[rsa] offset0:96 offset1:98\n"
      "ds_read2_b32 v[154:155], %[rsa] offset0:100 offset1:102\n"
      "ds_read2_b32 v[156:157], %[rsa] offset0:104 offset1:106\n"
      "ds_read2_b32 v[158:159], %[rsa] offset0:108 offset1:110\n"
      "v_mfma_f32_32x32x8_bf16 v[64:79], v[164:165], v[44:45], v[64:79]\n"
      "s_add_u32 s60, %[so], 20480\n"
      "s_add_u32 s61, %[so], 40960\n"
      "s_add_u32 s62, %[so], 61440\n"
      "v_mfma_f32_32x32x8_bf16 v[96:111], v[164:165], v[46:47], v[96:111]\n"
      "v_mfma_f32_32x32x8_bf16 v[80:95], v[166:167], v[44:45], v[80:95]\n"
      "v_mfma_f32_32x32x8_bf16 v[112:127], v[166:167], v[46:47], v[112:127]\n"
      "s_waitcnt lgkmcnt(0)\n"
      "s_nop 4\n"
      "v_mul_f32 v64, v64, v128\n"
      "v_mul_f32 v65, v65, v129\n"
      "v_mul_f32 v66, v66, v130\n"
      "v_mul_f32 v67, v67, v131\n"
      "v_mul_f32 v68, v68, v132\n"
      "v_mul_f32 v69, v69, v133\n"
      "v_mul_f32 v70, v70, v134\n"
      "v_mul_f32 v71, v71, v135\n"
      "v_cvt_pk_bf16_f32 v36, v64, v65\n"
      "v_cvt_pk_bf16_f32 v37, v66, v67\n"
      "v_cvt_pk_bf16_f32 v38, v68, v69\n"
      "v_cvt_pk_bf16_f32 v39, v70, v71\n"
      "buffer_store_dwordx4 v[36:39], %[vo], %[rs], %[so] offen offset:0 sc1\n"
      "v_mul_f32 v72, v72, v136\n"
      "v_mul_f32 v73, v73, v137\n"
      "v_mul_f32 v74, v74, v138\n"
      "v_mul_f32 v75, v75, v139\n"
      "v_mul_f32 v76, v76, v140\n"
      "v_mul_f32 v77, v77, v141\n"
      "v_mul_f32 v78, v78, v142\n"
      "v_mul_f32 v79, v79, v143\n"
      "v_cvt_pk_bf16_f32 v40, v72, v73\n"
      "v_cvt_pk_bf16_f32 v41, v74, v75\n"
      "v_cvt_pk_bf16_f32 v42, v76, v77\n"
      "v_cvt_pk_bf16_f32 v43, v78, v79\n"
      "buffer_store_dwordx4 v[40:43], %[vo], %[rs], s60 offen offset:0 sc1\n"
      "v_mul_f32 v96, v96, v128\n"
      "v_mul_f32 v97, v97, v129\n"
      "v_mul_f32 v98, v98, v130\n"
      "v_mul_f32 v99, v99, v131\n"
      "v_mul_f32 v100, v100, v132\n"
      "v_mul_f32 v101, v101, v133\n"
      "v_mul_f32 v102, v102, v134\n"
      "v_mul_f32 v103, v103, v135\n"
      "v_cvt_pk_bf16_f32 v36, v96, v97\n"
      "v_cvt_pk_bf16_f32 v37, v98, v99\n"
      "v_cvt_pk_bf16_f32 v38, v100, v101\n"
      "v_cvt_pk_bf16_f32 v39, v102, v103\n"
      "buffer_store_dwordx4 v[36:39], %[vo], %[rs], %[so] offen offset:512 sc1\n"
      "v_mul_f32 v104, v104, v136\n"
      "v_mul_f32 v105, v105, v137\n"
      "v_mul_f32 v106, v106, v138\n"
      "v_mul_f32 v107, v107, v139\n"
      "v_mul_f32 v108, v108, v140\n"
      "v_mul_f32 v109, v109, v141\n"
      "v_mul_f32 v110, v110, v142\n"
      "v_mul_f32 v111, v111, v143\n"
      "v_cvt_pk_bf16_f32 v40, v104, v105\n"
      "v_cvt_pk_bf16_f32 v41, v106, v107\n"
      "v_cvt_pk_bf16_f32 v42, v108, v109\n"
      "v_cvt_pk_bf16_f32 v43, v110, v111\n"
      "buffer_store_dwordx4 v[40:43], %[vo], %[rs], s60 offen offset:512 sc1\n"
      "v_mul_f32 v80, v80, v144\n"
      "v_mul_f32 v81, v81, v145\n"
      "v_mul_f32 v82, v82, v146\n"
      "v_mul_f32 v83, v83, v147\n"
      "v_mul_f32 v84, v84, v148\n"
      "v_mul_f32 v85, v85, v149\n"
      "v_mul_f32 v86, v86, v150\n"
      "v_mul_f32 v87, v87, v151\n"
      "v_cvt_pk_bf16_f32 v36, v80, v81\n"
      "v_cvt_pk_bf16_f32 v37, v82, v83\n"
      "v_cvt_pk_bf16_f32 v38, v84, v85\n"
      "v_cvt_pk_bf16_f32 v39, v86, v87\n"
      "buffer_store_dwordx4 v[36:39], %[vo], %[rs], s61 offen offset:0 sc1\n"
      "v_mul_f32 v88, v88, v152\n"
      "v_mul_f32 v89, v89, v153\n"
      "v_mul_f32 v90, v90, v154\n"
      "v_mul_f32 v91, v91, v155\n"
      "v_mul_f32 v92, v92, v156\n"
      "v_mul_f32 v93, v93, v157\n"
      "v_mul_f32 v94, v94, v158\n"
      "v_mul_f32 v95, v95, v159\n"
      "v_cvt_pk_bf16_f32 v40, v88, v89\n"
      "v_cvt_pk_bf16_f32 v41, v90, v91\n"
      "v_cvt_pk_bf16_f32 v42, v92, v93\n"
      "v_cvt_pk_bf16_f32 v43, v94, v95\n"
      "buffer_store_dwordx4 v[40:43], %[vo], %[rs], s62 offen offset:0 sc1\n"
      "v_mul_f32 v112, v112, v144\n"
      "v_mul_f32 v113, v113, v145\n"
      "v_mul_f32 v114, v114, v146\n"
      "v_mul_f32 v115, v115, v147\n"
      "v_mul_f32 v116, v116, v148\n"
      "v_mul_f32 v117, v117, v149\n"
      "v_mul_f32 v118, v118, v150\n"
      "v_mul_f32 v119, v119, v151\n"
      "v_cvt_pk_bf16_f32 v36, v112, v113\n"
      "v_cvt_pk_bf16_f32 v37, v114, v115\n"
      "v_cvt_pk_bf16_f32 v38, v116, v117\n"
      "v_cvt_pk_bf16_f32 v39, v118, v119\n"
      "buffer_store_dwordx4 v[36:39], %[vo], %[rs], s61 offen offset:512 sc1\n"
      "v_mul_f32 v120, v120, v152\n"
      "v_mul_f32 v121, v121, v153\n"
      "v_mul_f32 v122, v122, v154\n"
      "v_mul_f32 v123, v123, v155\n"
      "v_mul_f32 v124, v124, v156\n"
      "v_mul_f32 v125, v125, v157\n"
      "v_mul_f32 v126, v126, v158\n"
      "v_mul_f32 v127, v127, v159\n"
      "v_cvt_pk_bf16_f32 v40, v120, v121\n"
      "v_cvt_pk_bf16_f32 v41, v122, v123\n"
      "v_cvt_pk_bf16_f32 v42, v124, v125\n"
      "v_cvt_pk_bf16_f32 v43, v126, v127\n"
      "buffer_store_dwordx4 v[40:43], %[vo], %[rs], s62 offen offset:512 sc1\n"
      "s_setprio 0\n"
      "s_nop 0\n"
      "s_barrier\n"
            :  : G0_IN, G0_EP : "v36", "v37", "v38", "v39", "v40", "v41", "v42", "v43", "v44", "v45", "v46", "v47", "v48", "v49", "v50", "v51", "v52", "v53", "v54", "v55", "v56", "v57", "v58", "v59", "v60", "v61", "v62", "v63", "v64", "v65", "v66", "v67", "v68", "v69", "v70", "v71", "v72", "v73", "v74", "v75", "v76", "v77", "v78", "v79", "v80", "v81", "v82", "v83", "v84", "v85", "v86", "v87", "v88", "v89", "v90", "v91", "v92", "v93", "v94", "v95", "v96", "v97", "v98", "v99", "v100", "v101", "v102", "v103", "v104", "v105", "v106", "v107", "v108", "v109", "v110", "v111", "v112", "v113", "v114", "v115", "v116", "v117", "v118", "v119", "v120", "v121", "v122", "v123", "v124", "v125", "v126", "v127", "v128", "v129", "v130", "v131", "v132", "v133", "v134", "v135", "v136", "v137", "v138", "v139", "v140", "v141", "v142", "v143", "v144", "v145", "v146", "v147", "v148", "v149", "v150", "v151", "v152", "v153", "v154", "v155", "v156", "v157", "v158", "v159", "v160", "v161", "v162", "v163", "v164", "v165", "v166", "v167", "s60", "s61", "s62", "s63", "s64", "s65", "s66", "s67", "s68", "s69", "vcc", "scc", "memory");
        } else {
          asm volatile(
      "v_mov_b32 v52, %[xa]\n"
      "v_xor_b32 v53, 32, %[xa]\n"
      "v_xor_b32 v54, 64, %[xa]\n"
      "v_xor_b32 v55, 96, %[xa]\n"
      "v_mov_b32 v50, %[wa0]\n"
      "v_mov_b32 v51, %[wa1]\n"
      "v_mov_b32 v56, %[o0]\n"
      "v_mov_b32 v57, %[o0]\n"
      "v_mov_b32 v58, %[o1]\n"
      "v_mov_b32 v59, %[o1]\n"
      "v_mov_b32 v60, %[ix]\n"
      "v_mov_b32 v160, 0\n"
      "v_mov_b32 v161, 0\n"
      "v_mov_b32 v162, 0\n"
      "v_mov_b32 v163, 0\n"
      "v_mov_b32 v45, 0\n"
      "v_mov_b32 v47, 0\n"
      "s_nop 1\n"
      "v_mfma_f32_32x32x16_bf16 v[64:79], v[160:163], v[160:163], 0\n"
      "v_mfma_f32_32x32x16_bf16 v[80:95], v[160:163], v[160:163], 0\n"
      "v_mfma_f32_32x32x16_bf16 v[96:111], v[160:163], v[160:163], 0\n"
      "v_mfma_f32_32x32x16_bf16 v[112:127], v[160:163], v[160:163], 0\n"
      "s_mov_b32 s60, 4\n"
      "s_mov_b32 s61, 0\n"
      "s_mov_b32 s62, 10\n"
      "s_waitcnt lgkmcnt(0)\n"
      "s_barrier\n"
      "ds_read_b128 v[128:131], v52 offset:0\n"
      "ds_read_b64_tr_b16 v[136:137], v50 offset:0\n"
      "ds_read_b64_tr_b16 v[138:139], v50 offset:1024\n"
      "ds_read_b128 v[132:135], v52 offset:4096\n"
      "ds_read_b64_tr_b16 v[140:141], v51 offset:0\n"
      "ds_read_b64_tr_b16 v[142:143], v51 offset:1024\n"
      "Lp%=:\n"
      "s_waitcnt lgkmcnt(3)\n"
      "v_mfma_f32_32x32x16_bf16 v[64:79], v[136:139], v[128:131], v[64:79]\n"
      "ds_read_b128 v[144:147], v53 offset:0\n"
      "ds_read_b64_tr_b16 v[152:153], v50 offset:4096\n"
      "ds_read_b64_tr_b16 v[154:155], v50 offset:5120\n"
      "s_waitcnt lgkmcnt(3)\n"
      "v_mfma_f32_32x32x16_bf16 v[80:95], v[136:139], v[132:135], v[80:95]\n"
      "ds_read_b128 v[148:151], v53 offset:4096\n"
      "ds_read_b64_tr_b16 v[156:157], v51 offset:4096\n"
      "ds_read_b64_tr_b16 v[158:159], v51 offset:5120\n"
      "v_mfma_f32_32x32x16_bf16 v[96:111], v[140:143], v[128:131], v[96:111]\n"
      "s_add_u32 s60, s60, 1\n"
      "s_cmp_eq_u32 s60, 6\n"
      "s_mov_b32 s63, 16384\n"
      "s_cmov_b32 s63, 4294885376\n"
      "v_mfma_f32_32x32x16_bf16 v[112:127], v[140:143], v[132:135], v[112:127]\n"
      "s_cmov_b32 s60, 0\n"
      "v_smfmac_f32_16x16x64_bf16 v[160:163], v[56:59], v[136:143], v60\n"
      "s_waitcnt lgkmcnt(3)\n"
      "v_mfma_f32_32x32x16_bf16 v[64:79], v[152:155], v[144:147], v[64:79]\n"
      "ds_read_b128 v[128:131], v54 offset:0\n"
      "ds_read_b64_tr_b16 v[136:137], v50 offset:8192\n"
      "ds_read_b64_tr_b16 v[138:139], v50 offset:9216\n"
      "s_waitcnt lgkmcnt(3)\n"
      "v_mfma_f32_32x32x16_bf16 v[80:95], v[152:155], v[148:151], v[80:95]\n"
      "ds_read_b128 v[132:135], v54 offset:4096\n"
      "ds_read_b64_tr_b16 v[140:141], v51 offset:8192\n"
      "ds_read_b64_tr_b16 v[142:143], v51 offset:9216\n"
      "v_mfma_f32_32x32x16_bf16 v[96:111], v[156:159], v[144:147], v[96:111]\n"
      "s_add_u32 s61, s61, 1\n"
      "s_cmp_eq_u32 s61, 3\n"
      "s_mov_b32 s64, 16384\n"
      "s_cmov_b32 s64, 4294934528\n"
      "v_mfma_f32_32x32x16_bf16 v[112:127], v[156:159], v[148:151], v[112:127]\n"
      "s_cmov_b32 s61, 0\n"
      "v_smfmac_f32_16x16x64_bf16 v[160:163], v[56:59], v[152:159], v60\n"
      "s_waitcnt lgkmcnt(3)\n"
      "v_mfma_f32_32x32x16_bf16 v[64:79], v[136:139], v[128:131], v[64:79]\n"
      "ds_read_b128 v[144:147], v55 offset:0\n"
      "ds_read_b64_tr_b16 v[152:153], v50 offset:12288\n"
      "ds_read_b64_tr_b16 v[154:155], v50 offset:13312\n"
      "s_waitcnt lgkmcnt(3)\n"
      "v_mfma_f32_32x32x16_bf16 v[80:95], v[136:139], v[132:135], v[80:95]\n"
      "ds_read_b128 v[148:151], v55 offset:4096\n"
      "ds_read_b64_tr_b16 v[156:157], v51 offset:12288\n"
      "ds_read_b64_tr_b16 v[158:159], v51 offset:13312\n"
      "v_mfma_f32_32x32x16_bf16 v[96:111], v[140:143], v[128:131], v[96:111]\n"
      "v_add_u32 v52, s63, v52\n"
      "v_add_u32 v53, s63, v53\n"
      "v_add_u32 v54, s63, v54\n"
      "v_add_u32 v55, s63, v55\n"
      "v_mfma_f32_32x32x16_bf16 v[112:127], v[140:143], v[132:135], v[112:127]\n"
      "v_add_u32 v50, s64, v50\n"
      "v_add_u32 v51, s64, v51\n"
      "s_sub_u32 s62, s62, 1\n"
      "s_cmp_eq_u32 s62, 0\n"
      "v_smfmac_f32_16x16x64_bf16 v[160:163], v[56:59], v[136:143], v60\n"
      "s_waitcnt lgkmcnt(0)\n"
      "v_mfma_f32_32x32x16_bf16 v[64:79], v[152:155], v[144:147], v[64:79]\n"
      "s_barrier\n"
      "s_cbranch_scc1 Lx%=\n"
      "ds_read_b128 v[128:131], v52 offset:0\n"
      "ds_read_b64_tr_b16 v[136:137], v50 offset:0\n"
      "ds_read_b64_tr_b16 v[138:139], v50 offset:1024\n"
      "v_mfma_f32_32x32x16_bf16 v[80:95], v[152:155], v[148:151], v[80:95]\n"
      "ds_read_b128 v[132:135], v52 offset:4096\n"
      "ds_read_b64_tr_b16 v[140:141], v51 offset:0\n"
      "ds_read_b64_tr_b16 v[142:143], v51 offset:1024\n"
      "v_mfma_f32_32x32x16_bf16 v[96:111], v[156:159], v[144:147], v[96:111]\n"
      "v_mfma_f32_32x32x16_bf16 v[112:127], v[156:159], v[148:151], v[112:127]\n"
      "v_smfmac_f32_16x16x64_bf16 v[160:163], v[56:59], v[152:159], v60\n"
      "s_branch Lp%=\n"
      "Lx%=:\n"
      "s_setprio 2\n"
      "v_smfmac_f32_16x16x64_bf16 v[160:163], v[56:59], v[152:159], v60\n"
      "s_waitcnt vmcnt(0)\n"
      "ds_read2st64_b32 v[164:165], %[tfa] offset0:0 offset1:4\n"
      "ds_read2st64_b32 v[166:167], %[tfa] offset0:1 offset1:5\n"
      "ds_read2_b32 v[48:49], %[rsa] offset0:0 offset1:64\n"
      "v_mfma_f32_32x32x16_bf16 v[80:95], v[152:155], v[148:151], v[80:95]\n"
      "s_nop 3\n"
      "v_add_f32 v160, v160, v161\n"
      "v_add_f32 v162, v162, v163\n"
      "v_cvt_pk_bf16_f32 v161, v160, v160\n"
      "v_cvt_pk_bf16_f32 v163, v162, v162\n"
      "v_mfma_f32_32x32x16_bf16 v[96:111], v[156:159], v[144:147], v[96:111]\n"
      "v_and_b32 v161, %[hm], v161\n"
      "v_and_b32 v163, %[hm], v163\n"
      "v_sub_f32 v160, v160, v161\n"
      "v_sub_f32 v162, v162, v163\n"
      "v_cvt_pk_bf16_f32 v44, v160, v160\n"
      "v_cvt_pk_bf16_f32 v46, v162, v162\n"
      "s_waitcnt lgkmcnt(0)\n"
      "v_mfma_f32_32x32x16_bf16 v[112:127], v[156:159], v[148:151], v[112:127]\n"
      "v_mfma_f32_32x32x8_bf16 v[64:79], v[44:45], v[164:165], v[64:79]\n"
      "s_add_u32 s60, %[so], 20480\n"
      "s_add_u32 s61, %[so], 40960\n"
      "s_add_u32 s62, %[so], 61440\n"
      "v_mul_f32 v48, %[qs], v48\n"
      "v_mul_f32 v49, %[qs], v49\n"
      "v_mfma_f32_32x32x8_bf16 v[96:111], v[46:47], v[164:165], v[96:111]\n"
      "v_mfma_f32_32x32x8_bf16 v[80:95], v[44:45], v[166:167], v[80:95]\n"
      "v_mfma_f32_32x32x8_bf16 v[112:127], v[46:47], v[166:167], v[112:127]\n"
      "s_waitcnt lgkmcnt(0)\n"
      "s_nop 4\n"
      "v_mul_f32 v64, v64, v48\n"
      "v_mul_f32 v65, v65, v48\n"
      "v_mul_f32 v66, v66, v48\n"
      "v_mul_f32 v67, v67, v48\n"
      "v_mul_f32 v68, v68, v48\n"
      "v_mul_f32 v69, v69, v48\n"
      "v_mul_f32 v70, v70, v48\n"
      "v_mul_f32 v71, v71, v48\n"
      "v_cvt_pk_bf16_f32 v36, v64, v65\n"
      "v_cvt_pk_bf16_f32 v37, v66, v67\n"
      "v_cvt_pk_bf16_f32 v38, v68, v69\n"
      "v_cvt_pk_bf16_f32 v39, v70, v71\n"
      "buffer_store_dwordx4 v[36:39], %[vo], %[rs], %[so] offen offset:0 sc1\n"
      "v_mul_f32 v72, v72, v48\n"
      "v_mul_f32 v73, v73, v48\n"
      "v_mul_f32 v74, v74, v48\n"
      "v_mul_f32 v75, v75, v48\n"
      "v_mul_f32 v76, v76, v48\n"
      "v_mul_f32 v77, v77, v48\n"
      "v_mul_f32 v78, v78, v48\n"
      "v_mul_f32 v79, v79, v48\n"
      "v_cvt_pk_bf16_f32 v40, v72, v73\n"
      "v_cvt_pk_bf16_f32 v41, v74, v75\n"
      "v_cvt_pk_bf16_f32 v42, v76, v77\n"
      "v_cvt_pk_bf16_f32 v43, v78, v79\n"
      "buffer_store_dwordx4 v[40:43], %[vo], %[rs], %[so] offen offset:1024 sc1\n"
      "v_mul_f32 v96, v96, v48\n"
      "v_mul_f32 v97, v97, v48\n"
      "v_mul_f32 v98, v98, v48\n"
      "v_mul_f32 v99, v99, v48\n"
      "v_mul_f32 v100, v100, v48\n"
      "v_mul_f32 v101, v101, v48\n"
      "v_mul_f32 v102, v102, v48\n"
      "v_mul_f32 v103, v103, v48\n"
      "v_cvt_pk_bf16_f32 v36, v96, v97\n"
      "v_cvt_pk_bf16_f32 v37, v98, v99\n"
      "v_cvt_pk_bf16_f32 v38, v100, v101\n"
      "v_cvt_pk_bf16_f32 v39, v102, v103\n"
      "buffer_store_dwordx4 v[36:39], %[vo], %[rs], %[so] offen offset:2048 sc1\n"
      "v_mul_f32 v104, v104, v48\n"
      "v_mul_f32 v105, v105, v48\n"
      "v_mul_f32 v106, v106, v48\n"
      "v_mul_f32 v107, v107, v48\n"
      "v_mul_f32 v108, v108, v48\n"
      "v_mul_f32 v109, v109, v48\n"
      "v_mul_f32 v110, v110, v48\n"
      "v_mul_f32 v111, v111, v48\n"
      "v_cvt_pk_bf16_f32 v40, v104, v105\n"
      "v_cvt_pk_bf16_f32 v41, v106, v107\n"
      "v_cvt_pk_bf16_f32 v42, v108, v109\n"
      "v_cvt_pk_bf16_f32 v43, v110, v111\n"
      "buffer_store_dwordx4 v[40:43], %[vo], %[rs], %[so] offen offset:3072 sc1\n"
      "v_mul_f32 v80, v80, v49\n"
      "v_mul_f32 v81, v81, v49\n"
      "v_mul_f32 v82, v82, v49\n"
      "v_mul_f32 v83, v83, v49\n"
      "v_mul_f32 v84, v84, v49\n"
      "v_mul_f32 v85, v85, v49\n"
      "v_mul_f32 v86, v86, v49\n"
      "v_mul_f32 v87, v87, v49\n"
      "v_cvt_pk_bf16_f32 v36, v80, v81\n"
      "v_cvt_pk_bf16_f32 v37, v82, v83\n"
      "v_cvt_pk_bf16_f32 v38, v84, v85\n"
      "v_cvt_pk_bf16_f32 v39, v86, v87\n"
      "buffer_store_dwordx4 v[36:39], %[vo], %[rs], s61 offen offset:0 sc1\n"
      "v_mul_f32 v88, v88, v49\n"
      "v_mul_f32 v89, v89, v49\n"
      "v_mul_f32 v90, v90, v49\n"
      "v_mul_f32 v91, v91, v49\n"
      "v_mul_f32 v92, v92, v49\n"
      "v_mul_f32 v93, v93, v49\n"
      "v_mul_f32 v94, v94, v49\n"
      "v_mul_f32 v95, v95, v49\n"
      "v_cvt_pk_bf16_f32 v40, v88, v89\n"
      "v_cvt_pk_bf16_f32 v41, v90, v91\n"
      "v_cvt_pk_bf16_f32 v42, v92, v93\n"
      "v_cvt_pk_bf16_f32 v43, v94, v95\n"
      "buffer_store_dwordx4 v[40:43], %[vo], %[rs], s61 offen offset:1024 sc1\n"
      "v_mul_f32 v112, v112, v49\n"
      "v_mul_f32 v113, v113, v49\n"
      "v_mul_f32 v114, v114, v49\n"
      "v_mul_f32 v115, v115, v49\n"
      "v_mul_f32 v116, v116, v49\n"
      "v_mul_f32 v117, v117, v49\n"
      "v_mul_f32 v118, v118, v49\n"
      "v_mul_f32 v119, v119, v49\n"
      "v_cvt_pk_bf16_f32 v36, v112, v113\n"
      "v_cvt_pk_bf16_f32 v37, v114, v115\n"
      "v_cvt_pk_bf16_f32 v38, v116, v117\n"
      "v_cvt_pk_bf16_f32 v39, v118, v119\n"
      "buffer_store_dwordx4 v[36:39], %[vo], %[rs], s61 offen offset:2048 sc1\n"
      "v_mul_f32 v120, v120, v49\n"
      "v_mul_f32 v121, v121, v49\n"
      "v_mul_f32 v122, v122, v49\n"
      "v_mul_f32 v123, v123, v49\n"
      "v_mul_f32 v124, v124, v49\n"
      "v_mul_f32 v125, v125, v49\n"
      "v_mul_f32 v126, v126, v49\n"
      "v_mul_f32 v127, v127, v49\n"
      "v_cvt_pk_bf16_f32 v40, v120, v121\n"
      "v_cvt_pk_bf16_f32 v41, v122, v123\n"
      "v_cvt_pk_bf16_f32 v42, v124, v125\n"
      "v_cvt_pk_bf16_f32 v43, v126, v127\n"
      "buffer_store_dwordx4 v[40:43], %[vo], %[rs], s61 offen offset:3072 sc1\n"
      "s_setprio 0\n"
      "s_nop 0\n"
      "s_barrier\n"
            :  : G0_IN, G0_EP, [qs] "s"(ty == 0 ? 0.32259635f : 1.f) : "v36", "v37", "v38", "v39", "v40", "v41", "v42", "v43", "v44", "v45", "v46", "v47", "v48", "v49", "v50", "v51", "v52", "v53", "v54", "v55", "v56", "v57", "v58", "v59", "v60", "v61", "v62", "v63", "v64", "v65", "v66", "v67", "v68", "v69", "v70", "v71", "v72", "v73", "v74", "v75", "v76", "v77", "v78", "v79", "v80", "v81", "v82", "v83", "v84", "v85", "v86", "v87", "v88", "v89", "v90", "v91", "v92", "v93", "v94", "v95", "v96", "v97", "v98", "v99", "v100", "v101", "v102", "v103", "v104", "v105", "v106", "v107", "v108", "v109", "v110", "v111", "v112", "v113", "v114", "v115", "v116", "v117", "v118", "v119", "v120", "v121", "v122", "v123", "v124", "v125", "v126", "v127", "v128", "v129", "v130", "v131", "v132", "v133", "v134", "v135", "v136", "v137", "v138", "v139", "v140", "v141", "v142", "v143", "v144", "v145", "v146", "v147", "v148", "v149", "v150", "v151", "v152", "v153", "v154", "v155", "v156", "v157", "v158", "v159", "v160", "v161", "v162", "v163", "v164", "v165", "v166", "v167", "s60", "s61", "s62", "s63", "s64", "s65", "s66", "s67", "s68", "s69", "vcc", "scc", "memory");
        }
      }
      STAMP(1);
      const u32 blk = 2 * b5 + ni, fh = (64 * blk) / 20 + fresh_lane();
      const u32 fqw = sel((qc < 15) & (fh <= (64 * blk + 63) / 20), WS_FQ + fh * 2048 + ((ty * 32 + 2 * rb + mi) * 2 + blk - (20 * fh) / 64) * 8, WS_END);
      asm volatile("" :: "v"(fqw));
      vmwait<0>();
      __builtin_amdgcn_raw_buffer_store_b64(__builtin_bit_cast(u32x2, tag), rs, fqw, 0, SC1);
      STAMP(2);
#pragma unroll
      for (int i = 0; i < 5; ++i) { bar(); if (i == 0) attn_addr(); }
    } else {
      u32x4 xr[3];
      xr[0] = *(GAS const u32x4*)(x + xro);
      xr[1] = *(GAS const u32x4*)(x + xro + 512);
      xr[2] = *(GAS const u32x4*)(x + xro - g16 * 256 + 1024);
      tag = *epoch + 1;
      attn_addr();
      u32x8 ga;
      {
        const u32 xa = lbase + 65536 + (32 * w + tok) * 128 + ((hh ^ ((tok >> 1) & 7)) << 4);
        const u32 wa = lbase + 32768 + (8 * hh + q4) * 192 + (16 * g16 + 4 * (((p4 & 1) << 1) | (p4 >> 1))) * 2;
#pragma unroll
        for (u32 s = 0; s < 4; ++s) { ga[s] = wa + ((s + w) & 3) * 3072; ga[4 + s] = xa ^ (((s + w) & 3) << 5); }
      }
      u32x4 gon = {o0, o0, o1, o1};
      f32x8 gcs = {};
      asm volatile("" : "+{v[48:55]}"(ga), "+{v[32:35]}"(gon), "+{v[36:43]}"(gcs));
      bar();
      STAMP(2);
      __builtin_amdgcn_s_setprio(2);
      asm volatile("" : "+v"(xr[0]), "+v"(xr[1]), "+v"(xr[2]));
      {
        const float wt = tok < 16 ? 1.f : 0.f;
        float s2 = 0.f, s1 = 0.f;
#pragma unroll
        for (int j = 0; j < 4; ++j) { s1 += (lo16(xr[0][j]) + hi16(xr[0][j])) + (lo16(xr[1][j]) + hi16(xr[1][j])); s2 += lo16(xr[2][j]) + hi16(xr[2][j]); }
        const float mean = sum32(s1 + wt * s2) * (1.f / 640.f);
        float v1 = 0.f, v2 = 0.f;
#pragma unroll
        for (int j = 0; j < 4; ++j) {
          float d;
          d = lo16(xr[0][j]) - mean; v1 += d * d; d = hi16(xr[0][j]) - mean; v1 += d * d;
          d = lo16(xr[1][j]) - mean; v1 += d * d; d = hi16(xr[1][j]) - mean; v1 += d * d;
          d = lo16(xr[2][j]) - mean; v2 += d * d; d = hi16(xr[2][j]) - mean; v2 += d * d;
        }
        const float rstd = __builtin_amdgcn_rsqf(sum32(v1 + wt * v2) * (1.f / 640.f) + 1e-5f);
        const float sv = tok ? __builtin_amdgcn_rcpf(rstd) : -mean;
        const u32 sh = pk(sv, sv) & 0xffff0000u;
        __builtin_amdgcn_raw_buffer_store_b64((u32x2){(u32)tag, tok == 2 ? __builtin_bit_cast(u32, rstd) : pk(asf(sh), sv - asf(sh))}, rs,
                                              tok < 3 ? WS_ST + tok * 16384 + srow * 8 : WS_END, 0, SC1);
      }
      STAMP(1);
      __builtin_amdgcn_s_setprio(0);
      bar();
      f32x16 h0, h1, h2;
      asm volatile(
      "ds_read_b64_tr_b16 v[104:105], v48 offset:0\n"
      "ds_read_b64_tr_b16 v[106:107], v48 offset:768\n"
      "ds_read_b128 v[116:119], v52 offset:0\n"
      "ds_read_b64_tr_b16 v[108:109], v48 offset:64\n"
      "ds_read_b64_tr_b16 v[110:111], v48 offset:832\n"
      "ds_read_b64_tr_b16 v[112:113], v48 offset:128\n"
      "ds_read_b64_tr_b16 v[114:115], v48 offset:896\n"
      "ds_read_b64_tr_b16 v[120:121], v49 offset:0\n"
      "ds_read_b64_tr_b16 v[122:123], v49 offset:768\n"
      "ds_read_b128 v[132:135], v53 offset:0\n"
      "ds_read_b64_tr_b16 v[124:125], v49 offset:64\n"
      "ds_read_b64_tr_b16 v[126:127], v49 offset:832\n"
      "ds_read_b64_tr_b16 v[128:129], v49 offset:128\n"
      "ds_read_b64_tr_b16 v[130:131], v49 offset:896\n"
      "s_waitcnt lgkmcnt(7)\n"
      "v_mfma_f32_32x32x16_bf16 v[56:71], v[104:107], v[116:119], 0\n"
      "ds_read_b64_tr_b16 v[136:137], v50 offset:0\n"
      "ds_read_b64_tr_b16 v[138:139], v50 offset:768\n"
      "ds_read_b128 v[148:151], v54 offset:0\n"
      "v_mfma_f32_32x32x16_bf16 v[72:87], v[108:111], v[116:119], 0\n"
      "ds_read_b64_tr_b16 v[140:141], v50 offset:64\n"
      "ds_read_b64_tr_b16 v[142:143], v50 offset:832\n"
      "ds_read_b64_tr_b16 v[144:145], v50 offset:128\n"
      "ds_read_b64_tr_b16 v[146:147], v50 offset:896\n"
      "v_mfma_f32_32x32x16_bf16 v[88:103], v[112:115], v[116:119], 0\n"
      "v_smfmac_f32_16x16x64_bf16 v[36:39], v[32:35], v[104:111], v31\n"
      "v_smfmac_f32_16x16x64_bf16 v[40:43], v[32:35], v[112:119], v31\n"
      "s_waitcnt lgkmcnt(7)\n"
      "v_mfma_f32_32x32x16_bf16 v[56:71], v[120:123], v[132:135], v[56:71]\n"
      "ds_read_b64_tr_b16 v[152:153], v51 offset:0\n"
      "ds_read_b64_tr_b16 v[154:155], v51 offset:768\n"
      "ds_read_b128 v[164:167], v55 offset:0\n"
      "v_mfma_f32_32x32x16_bf16 v[72:87], v[124:127], v[132:135], v[72:87]\n"
      "ds_read_b64_tr_b16 v[156:157], v51 offset:64\n"
      "ds_read_b64_tr_b16 v[158:159], v51 offset:832\n"
      "ds_read_b64_tr_b16 v[160:161], v51 offset:128\n"
      "ds_read_b64_tr_b16 v[162:163], v51 offset:896\n"
      "v_mfma_f32_32x32x16_bf16 v[88:103], v[128:131], v[132:135], v[88:103]\n"
      "v_add_u32 v52, 16384, v52\n"
      "v_add_u32 v48, 16384, v48\n"
      "v_add_u32 v53, 16384, v53\n"
      "v_add_u32 v49, 16384, v49\n"
      "s_waitcnt lgkmcnt(7)\n"
      "v_mfma_f32_32x32x16_bf16 v[56:71], v[136:139], v[148:151], v[56:71]\n"
      "v_add_u32 v54, 16384, v54\n"
      "v_add_u32 v50, 16384, v50\n"
      "v_add_u32 v55, 16384, v55\n"
      "v_mfma_f32_32x32x16_bf16 v[72:87], v[140:143], v[148:151], v[72:87]\n"
      "v_add_u32 v51, 16384, v51\n"
      "v_mfma_f32_32x32x16_bf16 v[88:103], v[144:147], v[148:151], v[88:103]\n"
      "s_waitcnt lgkmcnt(0)\n"
      "s_barrier\n"
      "v_mfma_f32_32x32x16_bf16 v[56:71], v[152:155], v[164:167], v[56:71]\n"
      "v_mfma_f32_32x32x16_bf16 v[72:87], v[156:159], v[164:167], v[72:87]\n"
      "v_mfma_f32_32x32x16_bf16 v[88:103], v[160:163], v[164:167], v[88:103]\n"
      "ds_read_b64_tr_b16 v[104:105], v48 offset:0\n"
      "ds_read_b64_tr_b16 v[106:107], v48 offset:768\n"
      "ds_read_b128 v[116:119], v52 offset:0\n"
      "ds_read_b64_tr_b16 v[108:109], v48 offset:64\n"
      "ds_read_b64_tr_b16 v[110:111], v48 offset:832\n"
      "ds_read_b64_tr_b16 v[112:113], v48 offset:128\n"
      "ds_read_b64_tr_b16 v[114:115], v48 offset:896\n"
      "ds_read_b64_tr_b16 v[120:121], v49 offset:0\n"
      "ds_read_b64_tr_b16 v[122:123], v49 offset:768\n"
      "ds_read_b128 v[132:135], v53 offset:0\n"
      "ds_read_b64_tr_b16 v[124:125], v49 offset:64\n"
      "ds_read_b64_tr_b16 v[126:127], v49 offset:832\n"
      "ds_read_b64_tr_b16 v[128:129], v49 offset:128\n"
      "ds_read_b64_tr_b16 v[130:131], v49 offset:896\n"
      "s_waitcnt lgkmcnt(7)\n"
      "v_mfma_f32_32x32x16_bf16 v[56:71], v[104:107], v[116:119], v[56:71]\n"
      "ds_read_b64_tr_b16 v[136:137], v50 offset:0\n"
      "ds_read_b64_tr_b16 v[138:139], v50 offset:768\n"
      "ds_read_b128 v[148:151], v54 offset:0\n"
      "v_mfma_f32_32x32x16_bf16 v[72:87], v[108:111], v[116:119], v[72:87]\n"
      "ds_read_b64_tr_b16 v[140:141], v50 offset:64\n"
      "ds_read_b64_tr_b16 v[142:143], v50 offset:832\n"
      "ds_read_b64_tr_b16 v[144:145], v50 offset:128\n"
      "ds_read_b64_tr_b16 v[146:147], v50 offset:896\n"
      "v_mfma_f32_32x32x16_bf16 v[88:103], v[112:115], v[116:119], v[88:103]\n"
      "v_smfmac_f32_16x16x64_bf16 v[36:39], v[32:35], v[104:111], v31\n"
      "v_smfmac_f32_16x16x64_bf16 v[40:43], v[32:35], v[112:119], v31\n"
      "s_waitcnt lgkmcnt(7)\n"
      "v_mfma_f32_32x32x16_bf16 v[56:71], v[120:123], v[132:135], v[56:71]\n"
      "ds_read_b64_tr_b16 v[152:153], v51 offset:0\n"
      "ds_read_b64_tr_b16 v[154:155], v51 offset:768\n"
      "ds_read_b128 v[164:167], v55 offset:0\n"
      "v_mfma_f32_32x32x16_bf16 v[72:87], v[124:127], v[132:135], v[72:87]\n"
      "ds_read_b64_tr_b16 v[156:157], v51 offset:64\n"
      "ds_read_b64_tr_b16 v[158:159], v51 offset:832\n"
      "ds_read_b64_tr_b16 v[160:161], v51 offset:128\n"
      "ds_read_b64_tr_b16 v[162:163], v51 offset:896\n"
      "v_mfma_f32_32x32x16_bf16 v[88:103], v[128:131], v[132:135], v[88:103]\n"
      "v_add_u32 v52, 4294885376, v52\n"
      "v_add_u32 v48, 98304, v48\n"
      "v_add_u32 v53, 4294885376, v53\n"
      "v_add_u32 v49, 98304, v49\n"
      "s_waitcnt lgkmcnt(7)\n"
      "v_mfma_f32_32x32x16_bf16 v[56:71], v[136:139], v[148:151], v[56:71]\n"
      "v_add_u32 v54, 4294885376, v54\n"
      "v_add_u32 v50, 98304, v50\n"
      "v_add_u32 v55, 4294885376, v55\n"
      "v_mfma_f32_32x32x16_bf16 v[72:87], v[140:143], v[148:151], v[72:87]\n"
      "v_add_u32 v51, 98304, v51\n"
      "v_mfma_f32_32x32x16_bf16 v[88:103], v[144:147], v[148:151], v[88:103]\n"
      "s_waitcnt lgkmcnt(0)\n"
      "s_barrier\n"
      "v_mfma_f32_32x32x16_bf16 v[56:71], v[152:155], v[164:167], v[56:71]\n"
      "v_mfma_f32_32x32x16_bf16 v[72:87], v[156:159], v[164:167], v[72:87]\n"
      "v_mfma_f32_32x32x16_bf16 v[88:103], v[160:163], v[164:167], v[88:103]\n"
      "ds_read_b64_tr_b16 v[104:105], v48 offset:0\n"
      "ds_read_b64_tr_b16 v[106:107], v48 offset:768\n"
      "ds_read_b128 v[116:119], v52 offset:0\n"
      "ds_read_b64_tr_b16 v[108:109], v48 offset:64\n"
      "ds_read_b64_tr_b16 v[110:111], v48 offset:832\n"
      "ds_read_b64_tr_b16 v[112:113], v48 offset:128\n"
      "ds_read_b64_tr_b16 v[114:115], v48 offset:896\n"
      "ds_read_b64_tr_b16 v[120:121], v49 offset:0\n"
      "ds_read_b64_tr_b16 v[122:123], v49 offset:768\n"
      "ds_read_b128 v[132:135], v53 offset:0\n"
      "ds_read_b64_tr_b16 v[124:125], v49 offset:64\n"
      "ds_read_b64_tr_b16 v[126:127], v49 offset:832\n"
      "ds_read_b64_tr_b16 v[128:129], v49 offset:128\n"
      "ds_read_b64_tr_b16 v[130:131], v49 offset:896\n"
      "s_waitcnt lgkmcnt(7)\n"
      "v_mfma_f32_32x32x16_bf16 v[56:71], v[104:107], v[116:119], v[56:71]\n"
      "ds_read_b64_tr_b16 v[136:137], v50 offset:0\n"
      "ds_read_b64_tr_b16 v[138:139], v50 offset:768\n"
      "ds_read_b128 v[148:151], v54 offset:0\n"
      "v_mfma_f32_32x32x16_bf16 v[72:87], v[108:111], v[116:119], v[72:87]\n"
      "ds_read_b64_tr_b16 v[140:141], v50 offset:64\n"
      "ds_read_b64_tr_b16 v[142:143], v50 offset:832\n"
      "ds_read_b64_tr_b16 v[144:145], v50 offset:128\n"
      "ds_read_b64_tr_b16 v[146:147], v50 offset:896\n"
      "v_mfma_f32_32x32x16_bf16 v[88:103], v[112:115], v[116:119], v[88:103]\n"
      "v_smfmac_f32_16x16x64_bf16 v[36:39], v[32:35], v[104:111], v31\n"
      "v_smfmac_f32_16x16x64_bf16 v[40:43], v[32:35], v[112:119], v31\n"
      "s_waitcnt lgkmcnt(7)\n"
      "v_mfma_f32_32x32x16_bf16 v[56:71], v[120:123], v[132:135], v[56:71]\n"
      "ds_read_b64_tr_b16 v[152:153], v51 offset:0\n"
      "ds_read_b64_tr_b16 v[154:155], v51 offset:768\n"
      "ds_read_b128 v[164:167], v55 offset:0\n"
      "v_mfma_f32_32x32x16_bf16 v[72:87], v[124:127], v[132:135], v[72:87]\n"
      "ds_read_b64_tr_b16 v[156:157], v51 offset:64\n"
      "ds_read_b64_tr_b16 v[158:159], v51 offset:832\n"
      "ds_read_b64_tr_b16 v[160:161], v51 offset:128\n"
      "ds_read_b64_tr_b16 v[162:163], v51 offset:896\n"
      "v_mfma_f32_32x32x16_bf16 v[88:103], v[128:131], v[132:135], v[88:103]\n"
      "v_add_u32 v52, 32768, v52\n"
      "v_add_u32 v48, 0, v48\n"
      "v_add_u32 v53, 32768, v53\n"
      "v_add_u32 v49, 0, v49\n"
      "s_waitcnt lgkmcnt(7)\n"
      "v_mfma_f32_32x32x16_bf16 v[56:71], v[136:139], v[148:151], v[56:71]\n"
      "v_add_u32 v54, 32768, v54\n"
      "v_add_u32 v50, 0, v50\n"
      "v_add_u32 v55, 32768, v55\n"
      "v_mfma_f32_32x32x16_bf16 v[72:87], v[140:143], v[148:151], v[72:87]\n"
      "v_add_u32 v51, 0, v51\n"
      "v_mfma_f32_32x32x16_bf16 v[88:103], v[144:147], v[148:151], v[88:103]\n"
      "s_waitcnt lgkmcnt(0)\n"
      "s_barrier\n"
      "v_mfma_f32_32x32x16_bf16 v[56:71], v[152:155], v[164:167], v[56:71]\n"
      "v_mfma_f32_32x32x16_bf16 v[72:87], v[156:159], v[164:167], v[72:87]\n"
      "v_mfma_f32_32x32x16_bf16 v[88:103], v[160:163], v[164:167], v[88:103]\n"
      "s_nop 11\n"
        : "={v[56:71]}"(h0), "={v[72:87]}"(h1), "={v[88:103]}"(h2), "+{v[48:55]}"(ga), "+{v[36:43]}"(gcs) : "{v[32:35]}"(gon), "{v31}"(ix)
        : "v44", "v45", "v46", "v47", "v104", "v105", "v106", "v107", "v108", "v109", "v110", "v111", "v112", "v113", "v114", "v115", "v116", "v117", "v118", "v119", "v120", "v121", "v122", "v123", "v124", "v125", "v126", "v127", "v128", "v129", "v130", "v131", "v132", "v133", "v134", "v135", "v136", "v137", "v138", "v139", "v140", "v141", "v142", "v143", "v144", "v145", "v146", "v147", "v148", "v149", "v150", "v151", "v152", "v153", "v154", "v155", "v156", "v157", "v158", "v159", "v160", "v161", "v162", "v163", "v164", "v165", "v166", "v167", "s60", "s61", "s62", "s63", "s64", "s65", "s66", "s67", "s68", "s69", "s70", "s71", "vcc", "scc", "memory");
      const gptr bp = b1 + (96 * cb + tau(tok)) * 2;
      const u32 pl = tmin(w, 2u);
      const gptr sp = ws + WS_ST + pl * 16384 + rb * 1024;
      CVT_GEOMETRY(0 ? 2 * (b & ~1u) + w : b + 256 * (w >> 1))
      const gptr cva = cvb + (w & 1) * (16 + 2 * e8 + e16) * 1280;
      const u32 g_tf = lbase + L_ST + (32 * w + tok) * 8 + 4, g_cr = lbase + L_CP + tok * 16;
      const u32 bias0 = *(GAS const u16*)bp, bias1 = *(GAS const u16*)(bp + 64), bias2 = *(GAS const u16*)(bp + 128);
      const u32 warm = 0 ? *(GAS const u32*)(cvb + ((xcd & 1) ? 0 : (tok + e8 * (tok >> 3) + e16 * (tok >> 4)) * 1280 + hh * 128))
                                : *(GAS const u32*)(cva + ((lane & 15) + e8 * ((lane >> 3) & 1)) * 1280 + hh * 128);
#pragma unroll
      for (int i = 0; i < 1; ++i) bar();
      dma1_sc1(lane * 16, sp, lbase + L_ST + pl * 1024);
      bar(); bar(); bar();
      __builtin_amdgcn_s_setprio(2);
#pragma unroll 1
      for (u32 n = 0; n < SPIN_GLB; ++n) {
        vmwait<0>();
        const u32x4 t = *(LAS u32x4*)(L + L_ST + pl * 1024 + lane * 16);
        if (!__builtin_amdgcn_ballot_w64((t.x != (u32)tag) | (t.z != (u32)tag))) break;
        dma1_sc1(lane * 16, sp, lbase + L_ST + pl * 1024);
      }
      const u32 bm = hh ? 0u : 0x10001u, fb0 = bias0 * bm, fb1 = bias1 * bm, fb2 = bias2 * bm;
      asm volatile("" :: "v"(fb0), "v"(fb1), "v"(fb2), "v"(warm), "v"(g_tf), "v"(g_cr));
      __builtin_amdgcn_s_setprio(0);
#pragma unroll
      for (int i = 3 + 1; i < 5; ++i) bar();
      STAMP(3);
      asm volatile(
      "s_mov_b32 s70, 3256593\n"
      "s_mov_b32 s71, 0\n"
      "s_mov_b32 s62, 10\n"
      "s_mov_b32 s61, 0x03fe01fe\n"
      "s_waitcnt lgkmcnt(0)\n"
      "s_barrier\n"
      "ds_read_b64_tr_b16 v[104:105], v48 offset:0\n"
      "ds_read_b64_tr_b16 v[106:107], v48 offset:768\n"
      "ds_read_b128 v[116:119], v52 offset:0\n"
      "ds_read_b64_tr_b16 v[108:109], v48 offset:64\n"
      "ds_read_b64_tr_b16 v[110:111], v48 offset:832\n"
      "ds_read_b64_tr_b16 v[112:113], v48 offset:128\n"
      "ds_read_b64_tr_b16 v[114:115], v48 offset:896\n"
      "ds_read_b64_tr_b16 v[120:121], v49 offset:0\n"
      "ds_read_b64_tr_b16 v[122:123], v49 offset:768\n"
      "ds_read_b128 v[132:135], v53 offset:0\n"
      "ds_read_b64_tr_b16 v[124:125], v49 offset:64\n"
      "ds_read_b64_tr_b16 v[126:127], v49 offset:832\n"
      "ds_read_b64_tr_b16 v[128:129], v49 offset:128\n"
      "ds_read_b64_tr_b16 v[130:131], v49 offset:896\n"
      "s_waitcnt vmcnt(0)\n"
      "Lp%=:\n"
      "s_waitcnt lgkmcnt(7)\n"
      "v_mfma_f32_32x32x16_bf16 v[56:71], v[104:107], v[116:119], v[56:71]\n"
      "ds_read_b64_tr_b16 v[136:137], v50 offset:0\n"
      "ds_read_b64_tr_b16 v[138:139], v50 offset:768\n"
      "ds_read_b128 v[148:151], v54 offset:0\n"
      "v_mfma_f32_32x32x16_bf16 v[72:87], v[108:111], v[116:119], v[72:87]\n"
      "ds_read_b64_tr_b16 v[140:141], v50 offset:64\n"
      "ds_read_b64_tr_b16 v[142:143], v50 offset:832\n"
      "ds_read_b64_tr_b16 v[144:145], v50 offset:128\n"
      "ds_read_b64_tr_b16 v[146:147], v50 offset:896\n"
      "v_mfma_f32_32x32x16_bf16 v[88:103], v[112:115], v[116:119], v[88:103]\n"
      "s_bfe_i32 s63, s70, 0x40000\n"
      "s_lshl_b32 s63, s63, 14\n"
      "s_lshr_b64 s[70:71], s[70:71], 4\n"
      "v_smfmac_f32_16x16x64_bf16 v[36:39], v[32:35], v[104:111], v31\n"
      "s_sext_i32_i8 s64, s61\n"
      "s_lshl_b32 s64, s64, 14\n"
      "s_lshr_b32 s67, s61, 8\n"
      "v_smfmac_f32_16x16x64_bf16 v[40:43], v[32:35], v[112:119], v31\n"
      "s_lshl_b32 s61, s61, 24\n"
      "s_or_b32 s61, s61, s67\n"
      "s_waitcnt lgkmcnt(7)\n"
      "v_mfma_f32_32x32x16_bf16 v[56:71], v[120:123], v[132:135], v[56:71]\n"
      "ds_read_b64_tr_b16 v[152:153], v51 offset:0\n"
      "ds_read_b64_tr_b16 v[154:155], v51 offset:768\n"
      "ds_read_b128 v[164:167], v55 offset:0\n"
      "v_mfma_f32_32x32x16_bf16 v[72:87], v[124:127], v[132:135], v[72:87]\n"
      "ds_read_b64_tr_b16 v[156:157], v51 offset:64\n"
      "ds_read_b64_tr_b16 v[158:159], v51 offset:832\n"
      "ds_read_b64_tr_b16 v[160:161], v51 offset:128\n"
      "ds_read_b64_tr_b16 v[162:163], v51 offset:896\n"
      "v_mfma_f32_32x32x16_bf16 v[88:103], v[128:131], v[132:135], v[88:103]\n"
      "v_add_u32 v52, s63, v52\n"
      "v_add_u32 v48, s64, v48\n"
      "v_add_u32 v53, s63, v53\n"
      "v_add_u32 v49, s64, v49\n"
      "s_cmp_eq_u32 s62, 16\n"
      "s_cbranch_scc1 Lx%=\n"
      "s_waitcnt lgkmcnt(7)\n"
      "v_mfma_f32_32x32x16_bf16 v[56:71], v[136:139], v[148:151], v[56:71]\n"
      "v_add_u32 v54, s63, v54\n"
      "v_add_u32 v50, s64, v50\n"
      "v_add_u32 v55, s63, v55\n"
      "v_add_u32 v51, s64, v51\n"
      "s_cmp_eq_u32 s62, 11\n"
      "v_mfma_f32_32x32x16_bf16 v[72:87], v[140:143], v[148:151], v[72:87]\n"
      "s_waitcnt lgkmcnt(0)\n"
      "s_cbranch_scc1 Lk%=\n"
      "s_barrier\n"
      "Lk%=:\n"
      "ds_read_b64_tr_b16 v[104:105], v48 offset:0\n"
      "ds_read_b64_tr_b16 v[106:107], v48 offset:768\n"
      "ds_read_b128 v[116:119], v52 offset:0\n"
      "ds_read_b64_tr_b16 v[108:109], v48 offset:64\n"
      "v_mfma_f32_32x32x16_bf16 v[88:103], v[144:147], v[148:151], v[88:103]\n"
      "ds_read_b64_tr_b16 v[110:111], v48 offset:832\n"
      "ds_read_b64_tr_b16 v[112:113], v48 offset:128\n"
      "ds_read_b64_tr_b16 v[114:115], v48 offset:896\n"
      "v_mfma_f32_32x32x16_bf16 v[56:71], v[152:155], v[164:167], v[56:71]\n"
      "ds_read_b64_tr_b16 v[120:121], v49 offset:0\n"
      "ds_read_b64_tr_b16 v[122:123], v49 offset:768\n"
      "ds_read_b128 v[132:135], v53 offset:0\n"
      "v_mfma_f32_32x32x16_bf16 v[72:87], v[156:159], v[164:167], v[72:87]\n"
      "ds_read_b64_tr_b16 v[124:125], v49 offset:64\n"
      "ds_read_b64_tr_b16 v[126:127], v49 offset:832\n"
      "ds_read_b64_tr_b16 v[128:129], v49 offset:128\n"
      "ds_read_b64_tr_b16 v[130:131], v49 offset:896\n"
      "v_mfma_f32_32x32x16_bf16 v[88:103], v[160:163], v[164:167], v[88:103]\n"
      "s_add_u32 s62, s62, 1\n"
      "s_branch Lp%=\n"
      "Lx%=:\n"
      "v_add_f32 v36, v36, v37\n"
      "v_add_f32 v37, v38, v39\n"
      "v_add_f32 v38, v40, v41\n"
      "s_waitcnt lgkmcnt(7)\n"
      "v_mfma_f32_32x32x16_bf16 v[56:71], v[136:139], v[148:151], v[56:71]\n"
      "ds_write_b32 %[cw], v36 offset:0\n"
      "ds_write_b32 %[cw], v37 offset:512\n"
      "ds_write_b32 %[cw], v38 offset:1024\n"
      "ds_read2st64_b32 v[46:47], %[tfa] offset0:0 offset1:4\n"
      "v_mfma_f32_32x32x16_bf16 v[72:87], v[140:143], v[148:151], v[72:87]\n"
      "v_mfma_f32_32x32x16_bf16 v[88:103], v[144:147], v[148:151], v[88:103]\n"
      "s_waitcnt lgkmcnt(0)\n"
      "s_barrier\n"
      "ds_read_b128 v[104:107], %[cr] offset:0\n"
      "ds_read_b128 v[108:111], %[cr] offset:512\n"
      "ds_read_b128 v[112:115], %[cr] offset:1024\n"
      "v_mfma_f32_32x32x16_bf16 v[56:71], v[152:155], v[164:167], v[56:71]\n"
      "v_mov_b32 v37, %[b0]\n"
      "v_mov_b32 v39, %[b1]\n"
      "v_mov_b32 v41, %[b2]\n"
      "v_mfma_f32_32x32x16_bf16 v[72:87], v[156:159], v[164:167], v[72:87]\n"
      "v_mfma_f32_32x32x16_bf16 v[88:103], v[160:163], v[164:167], v[88:103]\n"
      "s_waitcnt lgkmcnt(2)\n"
      "v_add_f32 v104, v104, v105\n"
      "v_add_f32 v106, v106, v107\n"
      "v_add_f32 v104, v104, v106\n"
      "v_cvt_pk_bf16_f32 v105, v104, v104\n"
      "v_and_b32 v105, %[hm], v105\n"
      "v_sub_f32 v104, v104, v105\n"
      "v_cvt_pk_bf16_f32 v36, v104, v104\n"
      "s_waitcnt lgkmcnt(1)\n"
      "v_add_f32 v108, v108, v109\n"
      "v_add_f32 v110, v110, v111\n"
      "v_add_f32 v108, v108, v110\n"
      "v_cvt_pk_bf16_f32 v109, v108, v108\n"
      "v_and_b32 v109, %[hm], v109\n"
      "v_sub_f32 v108, v108, v109\n"
      "v_cvt_pk_bf16_f32 v38, v108, v108\n"
      "v_mfma_f32_32x32x8_bf16 v[56:71], v[36:37], v[46:47], v[56:71]\n"
      "s_waitcnt lgkmcnt(0)\n"
      "v_add_f32 v112, v112, v113\n"
      "v_add_f32 v114, v114, v115\n"
      "v_add_f32 v112, v112, v114\n"
      "v_cvt_pk_bf16_f32 v113, v112, v112\n"
      "v_and_b32 v113, %[hm], v113\n"
      "v_sub_f32 v112, v112, v113\n"
      "v_cvt_pk_bf16_f32 v40, v112, v112\n"
      "v_mfma_f32_32x32x8_bf16 v[72:87], v[38:39], v[46:47], v[72:87]\n"
      "s_nop 0\n"
      "v_mfma_f32_32x32x8_bf16 v[88:103], v[40:41], v[46:47], v[88:103]\n"
      "s_nop 0\n"
      "v_max_f32 v56, 0, v56\n"
      "v_max_f32 v57, 0, v57\n"
      "v_max_f32 v58, 0, v58\n"
      "v_max_f32 v59, 0, v59\n"
      "v_max_f32 v60, 0, v60\n"
      "v_max_f32 v61, 0, v61\n"
      "v_max_f32 v62, 0, v62\n"
      "v_max_f32 v63, 0, v63\n"
      "v_mul_f32 v56, v56, v56\n"
      "v_mul_f32 v57, v57, v57\n"
      "v_mul_f32 v58, v58, v58\n"
      "v_mul_f32 v59, v59, v59\n"
      "v_mul_f32 v60, v60, v60\n"
      "v_mul_f32 v61, v61, v61\n"
      "v_mul_f32 v62, v62, v62\n"
      "v_mul_f32 v63, v63, v63\n"
      "v_cvt_pk_bf16_f32 v120, v56, v57\n"
      "v_cvt_pk_bf16_f32 v121, v58, v59\n"
      "v_cvt_pk_bf16_f32 v122, v60, v61\n"
      "v_cvt_pk_bf16_f32 v123, v62, v63\n"
      "ds_write_b128 %[ha], v[120:123] offset:0\n"
      "v_max_f32 v64, 0, v64\n"
      "v_max_f32 v65, 0, v65\n"
      "v_max_f32 v66, 0, v66\n"
      "v_max_f32 v67, 0, v67\n"
      "v_max_f32 v68, 0, v68\n"
      "v_max_f32 v69, 0, v69\n"
      "v_max_f32 v70, 0, v70\n"
      "v_max_f32 v71, 0, v71\n"
      "v_mul_f32 v64, v64, v64\n"
      "v_mul_f32 v65, v65, v65\n"
      "v_mul_f32 v66, v66, v66\n"
      "v_mul_f32 v67, v67, v67\n"
      "v_mul_f32 v68, v68, v68\n"
      "v_mul_f32 v69, v69, v69\n"
      "v_mul_f32 v70, v70, v70\n"
      "v_mul_f32 v71, v71, v71\n"
      "v_cvt_pk_bf16_f32 v124, v64, v65\n"
      "v_cvt_pk_bf16_f32 v125, v66, v67\n"
      "v_cvt_pk_bf16_f32 v126, v68, v69\n"
      "v_cvt_pk_bf16_f32 v127, v70, v71\n"
      "ds_write_b128 %[ha], v[124:127] offset:1024\n"
      "v_max_f32 v72, 0, v72\n"
      "v_max_f32 v73, 0, v73\n"
      "v_max_f32 v74, 0, v74\n"
      "v_max_f32 v75, 0, v75\n"
      "v_max_f32 v76, 0, v76\n"
      "v_max_f32 v77, 0, v77\n"
      "v_max_f32 v78, 0, v78\n"
      "v_max_f32 v79, 0, v79\n"
      "v_mul_f32 v72, v72, v72\n"
      "v_mul_f32 v73, v73, v73\n"
      "v_mul_f32 v74, v74, v74\n"
      "v_mul_f32 v75, v75, v75\n"
      "v_mul_f32 v76, v76, v76\n"
      "v_mul_f32 v77, v77, v77\n"
      "v_mul_f32 v78, v78, v78\n"
      "v_mul_f32 v79, v79, v79\n"
      "v_cvt_pk_bf16_f32 v128, v72, v73\n"
      "v_cvt_pk_bf16_f32 v129, v74, v75\n"
      "v_cvt_pk_bf16_f32 v130, v76, v77\n"
      "v_cvt_pk_bf16_f32 v131, v78, v79\n"
      "ds_write_b128 %[ha], v[128:131] offset:2048\n"
      "v_max_f32 v80, 0, v80\n"
      "v_max_f32 v81, 0, v81\n"
      "v_max_f32 v82, 0, v82\n"
      "v_max_f32 v83, 0, v83\n"
      "v_max_f32 v84, 0, v84\n"
      "v_max_f32 v85, 0, v85\n"
      "v_max_f32 v86, 0, v86\n"
      "v_max_f32 v87, 0, v87\n"
      "v_mul_f32 v80, v80, v80\n"
      "v_mul_f32 v81, v81, v81\n"
      "v_mul_f32 v82, v82, v82\n"
      "v_mul_f32 v83, v83, v83\n"
      "v_mul_f32 v84, v84, v84\n"
      "v_mul_f32 v85, v85, v85\n"
      "v_mul_f32 v86, v86, v86\n"
      "v_mul_f32 v87, v87, v87\n"
      "v_cvt_pk_bf16_f32 v132, v80, v81\n"
      "v_cvt_pk_bf16_f32 v133, v82, v83\n"
      "v_cvt_pk_bf16_f32 v134, v84, v85\n"
      "v_cvt_pk_bf16_f32 v135, v86, v87\n"
      "ds_write_b128 %[ha], v[132:135] offset:3072\n"
      "v_max_f32 v88, 0, v88\n"
      "v_max_f32 v89, 0, v89\n"
      "v_max_f32 v90, 0, v90\n"
      "v_max_f32 v91, 0, v91\n"
      "v_max_f32 v92, 0, v92\n"
      "v_max_f32 v93, 0, v93\n"
      "v_max_f32 v94, 0, v94\n"
      "v_max_f32 v95, 0, v95\n"
      "v_mul_f32 v88, v88, v88\n"
      "v_mul_f32 v89, v89, v89\n"
      "v_mul_f32 v90, v90, v90\n"
      "v_mul_f32 v91, v91, v91\n"
      "v_mul_f32 v92, v92, v92\n"
      "v_mul_f32 v93, v93, v93\n"
      "v_mul_f32 v94, v94, v94\n"
      "v_mul_f32 v95, v95, v95\n"
      "v_cvt_pk_bf16_f32 v136, v88, v89\n"
      "v_cvt_pk_bf16_f32 v137, v90, v91\n"
      "v_cvt_pk_bf16_f32 v138, v92, v93\n"
      "v_cvt_pk_bf16_f32 v139, v94, v95\n"
      "ds_write_b128 %[ha], v[136:139] offset:4096\n"
      "v_max_f32 v96, 0, v96\n"
      "v_max_f32 v97, 0, v97\n"
      "v_max_f32 v98, 0, v98\n"
      "v_max_f32 v99, 0, v99\n"
      "v_max_f32 v100, 0, v100\n"
      "v_max_f32 v101, 0, v101\n"
      "v_max_f32 v102, 0, v102\n"
      "v_max_f32 v103, 0, v103\n"
      "v_mul_f32 v96, v96, v96\n"
      "v_mul_f32 v97, v97, v97\n"
      "v_mul_f32 v98, v98, v98\n"
      "v_mul_f32 v99, v99, v99\n"
      "v_mul_f32 v100, v100, v100\n"
      "v_mul_f32 v101, v101, v101\n"
      "v_mul_f32 v102, v102, v102\n"
      "v_mul_f32 v103, v103, v103\n"
      "v_cvt_pk_bf16_f32 v140, v96, v97\n"
      "v_cvt_pk_bf16_f32 v141, v98, v99\n"
      "v_cvt_pk_bf16_f32 v142, v100, v101\n"
      "v_cvt_pk_bf16_f32 v143, v102, v103\n"
      "ds_write_b128 %[ha], v[140:143] offset:5120\n"
        : "+{v[56:71]}"(h0), "+{v[72:87]}"(h1), "+{v[88:103]}"(h2), "+{v[48:55]}"(ga), "+{v[36:43]}"(gcs)
        : "{v[32:35]}"(gon), "{v31}"(ix), [hm] "v"(hm), [tfa] "v"(g_tf), [cr] "v"(g_cr), [cw] "v"(g_cr + 4 * w),
          [ha] "v"(lbase + L_H + w * 6144 + lane * 16), [b0] "v"(fb0), [b1] "v"(fb1), [b2] "v"(fb2)
        : "v44", "v45", "v46", "v47", "v104", "v105", "v106", "v107", "v108", "v109", "v110", "v111", "v112", "v113", "v114", "v115", "v116", "v117", "v118", "v119", "v120", "v121", "v122", "v123", "v124", "v125", "v126", "v127", "v128", "v129", "v130", "v131", "v132", "v133", "v134", "v135", "v136", "v137", "v138", "v139", "v140", "v141", "v142", "v143", "v144", "v145", "v146", "v147", "v148", "v149", "v150", "v151", "v152", "v153", "v154", "v155", "v156", "v157", "v158", "v159", "v160", "v161", "v162", "v163", "v164", "v165", "v166", "v167", "s60", "s61", "s62", "s63", "s64", "s65", "s66", "s67", "s68", "s69", "s70", "s71", "vcc", "scc", "memory");
    }
    STAMP(4);
    {
      u64 bad;
      __builtin_amdgcn_s_waitcnt(0x0f70);
      asm volatile(
      "s_setprio 3\n"
      "s_mov_b32 s62, 1\n"
      "s_mov_b32 s60, 0x01010102\n"
      "s_mov_b32 s61, 0x00fffb01\n"
      "s_mov_b32 s66, 0x400000\n"
      "v_mov_b32 v31, %[lk]\n"
      "v_mov_b32 v30, %[lr]\n"
      "v_mov_b32 v29, %[lv]\n"
      "v_mov_b32 v28, 0xdddddddd\n"
      "v_mov_b32 v24, %[sr]\n"
      "s_mov_b32 s70, 0xfffff\n"
      "s_mov_b32 s71, 0xfffff\n"
      "v_mov_b32 v48, %[one]\n"
      "v_mov_b32 v49, %[one]\n"
      "v_mov_b32 v50, %[one]\n"
      "v_mov_b32 v51, %[one]\n"
      "v_mov_b32 v52, %[one]\n"
      "v_mov_b32 v53, %[one]\n"
      "v_mov_b32 v54, %[one]\n"
      "v_mov_b32 v55, %[one]\n"
      "v_mov_b32 v160, 0\n"
      "v_mov_b32 v161, 0\n"
      "v_mov_b32 v162, 0\n"
      "v_mov_b32 v163, 0\n"
      "v_mov_b32 v164, 0\n"
      "v_mov_b32 v165, 0\n"
      "v_mov_b32 v166, 0\n"
      "v_mov_b32 v167, 0\n"
      "v_subrev_u32 v26, %[gk], v31\n"
      "v_lshrrev_b32 v27, 1, %[gk]\n"
      "v_sub_u32 v27, v30, v27\n"
      "v_mfma_f32_32x32x16_bf16 v[72:87], v[160:163], v[160:163], 0\n"
      "v_mfma_f32_32x32x16_bf16 v[56:71], v[160:163], v[160:163], 0\n"
      "s_waitcnt lgkmcnt(0)\n"
      "s_barrier\n"
      "ds_read_b128 v[36:39], %[lq]\n"
      "ds_read_b128 v[136:139], v26 offset:23040\n"
      "ds_read_b128 v[32:35], %[lqb]\n"
      "ds_read_b64 v[40:41], %[lqr]\n"
      "ds_read_b64 v[140:141], v27 offset:25088\n"
      "ds_read_b128 v[144:147], v31 offset:23040\n"
      "ds_read_b64 v[142:143], v30 offset:25088\n"
      "ds_read_b128 v[44:47], v31 offset:26112\n"
      "ds_read_b64 v[42:43], v30 offset:28160\n"
      "s_mov_b64 exec, s[70:71]\n"
      "ds_read_b128 v[48:51], v29 offset:29824\n"
      "s_mov_b64 exec, -1\n"
      "s_waitcnt lgkmcnt(8)\n"
      "v_mfma_f32_32x32x16_bf16 v[104:119], v[136:139], v[36:39], 0\n"
      "s_waitcnt lgkmcnt(7)\n"
      "v_mfma_f32_32x32x16_bf16 v[120:135], v[136:139], v[32:35], 0\n"
      "s_nop 8\n"
      "s_waitcnt lgkmcnt(5)\n"
      "v_mfma_f32_32x32x4_2b_bf16 v[104:135], v[140:141], v[40:41], v[104:135]\n"
      "s_nop 15\n"
      "s_nop 3\n"
      "v_max3_f32 v27, v104, v105, v106\n"
      "v_max3_f32 v25, v107, v108, v109\n"
      "v_max3_f32 v27, v27, v110, v111\n"
      "v_max3_f32 v25, v25, v112, v113\n"
      "v_max3_f32 v27, v27, v114, v115\n"
      "v_max3_f32 v25, v25, v116, v117\n"
      "v_max3_f32 v27, v27, v118, v119\n"
      "v_max3_f32 v25, v25, v120, v121\n"
      "v_max3_f32 v27, v27, v122, v123\n"
      "v_max3_f32 v25, v25, v124, v125\n"
      "v_max3_f32 v27, v27, v126, v127\n"
      "v_max3_f32 v25, v25, v128, v129\n"
      "v_max3_f32 v27, v27, v130, v131\n"
      "v_max3_f32 v25, v25, v132, v133\n"
      "v_max3_f32 v27, v27, v134, v135\n"
      "v_max_f32 v27, v27, v25\n"
      "v_mov_b32 v25, v27\n"
      "s_nop 1\n"
      "v_permlane32_swap_b32 v27, v25\n"
      "s_nop 1\n"
      "v_min_f32 v88, -v27, -v25\n"
      "v_mov_b32 v89, v88\n"
      "v_mov_b32 v90, v88\n"
      "v_mov_b32 v91, v88\n"
      "v_mov_b32 v92, v88\n"
      "v_mov_b32 v93, v88\n"
      "v_mov_b32 v94, v88\n"
      "v_mov_b32 v95, v88\n"
      "v_mov_b32 v96, v88\n"
      "v_mov_b32 v97, v88\n"
      "v_mov_b32 v98, v88\n"
      "v_mov_b32 v99, v88\n"
      "v_mov_b32 v100, v88\n"
      "v_mov_b32 v101, v88\n"
      "v_mov_b32 v102, v88\n"
      "v_mov_b32 v103, v88\n"
      "ds_read_b32 v27, v24\n"
      "s_waitcnt lgkmcnt(1)\n"
      "v_mfma_f32_32x32x16_bf16 v[104:119], v[144:147], v[36:39], v[88:103]\n"
      "v_mfma_f32_32x32x16_bf16 v[120:135], v[144:147], v[32:35], v[88:103]\n"
      "s_nop 9\n"
      "v_mfma_f32_32x32x4_2b_bf16 v[104:135], v[142:143], v[40:41], v[104:135]\n"
      "s_waitcnt lgkmcnt(0)\n"
      "s_nop 15\n"
      "s_nop 1\n"
      "Lloop%=:\n"
      "s_bitcmp1_b32 840, s62\n"
      "s_cselect_b32 s65, s62, 1\n"
      "v_exp_f32 v104, v104\n"
      "v_exp_f32 v105, v105\n"
      "s_waitcnt lgkmcnt(1)\n"
      "v_cmp_ne_u32 vcc, %[tg], v27\n"
      "s_cbranch_vccnz Lsa%=\n"
      "Lba%=:\n"
      "s_sub_u32 s65, s65, 1\n"
      "v_cmp_gt_u32 vcc, s65, v25\n"
      "v_smfmac_f32_32x32x32_bf16 v[56:71], v[52:55], v[160:167], v28\n"
      "s_mov_b64 exec, s[70:71]\n"
      "ds_read_b128 v[52:55], v29 offset:12288\n"
      "s_mov_b64 exec, -1\n"
      "s_bitcmp1_b32 vcc_lo, %[mt]\n"
      "s_cselect_b32 s63, 1, 0\n"
      "s_cbranch_scc1 Ly0_%=\n"
      "s_setprio 2\n"
      "Ly0_%=:\n"
      "v_exp_f32 v106, v106\n"
      "v_exp_f32 v107, v107\n"
      "v_exp_f32 v108, v108\n"
      "v_exp_f32 v109, v109\n"
      "v_mfma_f32_32x32x16_bf16 v[136:151], v[44:47], v[36:39], v[88:103]\n"
      "v_exp_f32 v110, v110\n"
      "v_exp_f32 v111, v111\n"
      "v_exp_f32 v120, v120\n"
      "v_exp_f32 v121, v121\n"
      "v_mfma_f32_32x32x16_bf16 v[152:167], v[44:47], v[32:35], v[88:103]\n"
      "ds_read_b128 v[44:47], v31 offset:0\n"
      "v_exp_f32 v122, v122\n"
      "v_exp_f32 v123, v123\n"
      "v_exp_f32 v124, v124\n"
      "v_exp_f32 v125, v125\n"
      "s_waitcnt lgkmcnt(2)\n"
      "s_bitcmp1_b32 420, s62\n"
      "s_cbranch_scc0 Lnw%=\n"
      "v_mov_b32 v26, s62\n"
      "ds_write_b32 %[sw], v26\n"
      "Lnw%=:\n"
      "v_smfmac_f32_32x32x32_bf16 v[72:87], v[48:51], v[104:111], v28\n"
      "v_exp_f32 v126, v126\n"
      "v_exp_f32 v127, v127\n"
      "v_exp_f32 v112, v112\n"
      "v_exp_f32 v113, v113\n"
      "v_mfma_f32_32x32x4_2b_bf16 v[136:167], v[42:43], v[40:41], v[136:167]\n"
      "ds_read_b64 v[42:43], v30 offset:2048\n"
      "v_exp_f32 v114, v114\n"
      "v_exp_f32 v115, v115\n"
      "v_exp_f32 v116, v116\n"
      "v_exp_f32 v117, v117\n"
      "v_exp_f32 v118, v118\n"
      "v_exp_f32 v119, v119\n"
      "v_exp_f32 v128, v128\n"
      "v_exp_f32 v129, v129\n"
      "v_smfmac_f32_32x32x32_bf16 v[56:71], v[48:51], v[120:127], v28\n"
      "s_mov_b64 exec, s[70:71]\n"
      "ds_read_b128 v[48:51], v29 offset:12928\n"
      "s_mov_b64 exec, -1\n"
      "v_exp_f32 v130, v130\n"
      "v_exp_f32 v131, v131\n"
      "v_exp_f32 v132, v132\n"
      "v_exp_f32 v133, v133\n"
      "s_waitcnt lgkmcnt(3)\n"
      "v_smfmac_f32_32x32x32_bf16 v[72:87], v[52:55], v[112:119], v28\n"
      "v_exp_f32 v134, v134\n"
      "v_exp_f32 v135, v135\n"
      "s_cmp_eq_u32 s63, 0\n"
      "s_cbranch_scc0 Ly1_%=\n"
      "s_setprio 2\n"
      "Ly1_%=:\n"
      "v_exp_f32 v136, v136\n"
      "v_exp_f32 v137, v137\n"
      "s_waitcnt lgkmcnt(1)\n"
      "v_smfmac_f32_32x32x32_bf16 v[56:71], v[52:55], v[128:135], v28\n"
      "s_mov_b64 exec, s[70:71]\n"
      "ds_read_b128 v[52:55], v29 offset:14848\n"
      "s_mov_b64 exec, -1\n"
      "v_exp_f32 v138, v138\n"
      "v_exp_f32 v139, v139\n"
      "v_exp_f32 v140, v140\n"
      "v_exp_f32 v141, v141\n"
      "v_mfma_f32_32x32x16_bf16 v[104:119], v[44:47], v[36:39], v[88:103]\n"
      "v_exp_f32 v142, v142\n"
      "v_exp_f32 v143, v143\n"
      "v_exp_f32 v152, v152\n"
      "v_exp_f32 v153, v153\n"
      "v_mfma_f32_32x32x16_bf16 v[120:135], v[44:47], v[32:35], v[88:103]\n"
      "ds_read_b128 v[44:47], v31 offset:3072\n"
      "v_exp_f32 v154, v154\n"
      "v_exp_f32 v155, v155\n"
      "v_exp_f32 v156, v156\n"
      "v_exp_f32 v157, v157\n"
      "s_waitcnt lgkmcnt(2)\n"
      "v_smfmac_f32_32x32x32_bf16 v[72:87], v[48:51], v[136:143], v28\n"
      "v_exp_f32 v158, v158\n"
      "v_exp_f32 v159, v159\n"
      "v_exp_f32 v144, v144\n"
      "v_exp_f32 v145, v145\n"
      "v_mfma_f32_32x32x4_2b_bf16 v[104:135], v[42:43], v[40:41], v[104:135]\n"
      "ds_read_b64 v[42:43], v30 offset:5120\n"
      "v_exp_f32 v146, v146\n"
      "v_exp_f32 v147, v147\n"
      "v_exp_f32 v148, v148\n"
      "v_exp_f32 v149, v149\n"
      "v_exp_f32 v150, v150\n"
      "v_exp_f32 v151, v151\n"
      "v_exp_f32 v160, v160\n"
      "v_exp_f32 v161, v161\n"
      "v_smfmac_f32_32x32x32_bf16 v[56:71], v[48:51], v[152:159], v28\n"
      "s_mov_b64 exec, s[70:71]\n"
      "ds_read_b128 v[48:51], v29 offset:15488\n"
      "s_mov_b64 exec, -1\n"
      "v_exp_f32 v162, v162\n"
      "v_exp_f32 v163, v163\n"
      "v_exp_f32 v164, v164\n"
      "v_exp_f32 v165, v165\n"
      "s_waitcnt lgkmcnt(3)\n"
      "v_smfmac_f32_32x32x32_bf16 v[72:87], v[52:55], v[144:151], v28\n"
      "v_exp_f32 v166, v166\n"
      "v_exp_f32 v167, v167\n"
      "s_cmp_eq_u32 s63, 0\n"
      "s_cbranch_scc0 Ly2_%=\n"
      "s_setprio 1\n"
      "Ly2_%=:\n"
      "v_exp_f32 v104, v104\n"
      "v_exp_f32 v105, v105\n"
      "s_waitcnt lgkmcnt(1)\n"
      "v_smfmac_f32_32x32x32_bf16 v[56:71], v[52:55], v[160:167], v28\n"
      "s_mov_b64 exec, s[70:71]\n"
      "ds_read_b128 v[52:55], v29 offset:17408\n"
      "s_mov_b64 exec, -1\n"
      "v_exp_f32 v106, v106\n"
      "v_exp_f32 v107, v107\n"
      "v_exp_f32 v108, v108\n"
      "v_exp_f32 v109, v109\n"
      "v_mfma_f32_32x32x16_bf16 v[136:151], v[44:47], v[36:39], v[88:103]\n"
      "v_exp_f32 v110, v110\n"
      "v_exp_f32 v111, v111\n"
      "v_exp_f32 v120, v120\n"
      "v_exp_f32 v121, v121\n"
      "v_mfma_f32_32x32x16_bf16 v[152:167], v[44:47], v[32:35], v[88:103]\n"
      "ds_read_b128 v[44:47], v31 offset:6144\n"
      "v_exp_f32 v122, v122\n"
      "v_exp_f32 v123, v123\n"
      "v_exp_f32 v124, v124\n"
      "v_exp_f32 v125, v125\n"
      "s_waitcnt lgkmcnt(2)\n"
      "v_smfmac_f32_32x32x32_bf16 v[72:87], v[48:51], v[104:111], v28\n"
      "v_add_u32 v24, 16, v24\n"
      "v_exp_f32 v126, v126\n"
      "v_exp_f32 v127, v127\n"
      "v_exp_f32 v112, v112\n"
      "v_exp_f32 v113, v113\n"
      "v_mfma_f32_32x32x4_2b_bf16 v[136:167], v[42:43], v[40:41], v[136:167]\n"
      "ds_read_b64 v[42:43], v30 offset:8192\n"
      "v_exp_f32 v114, v114\n"
      "v_exp_f32 v115, v115\n"
      "v_exp_f32 v116, v116\n"
      "v_exp_f32 v117, v117\n"
      "v_exp_f32 v118, v118\n"
      "v_exp_f32 v119, v119\n"
      "v_exp_f32 v128, v128\n"
      "v_exp_f32 v129, v129\n"
      "v_smfmac_f32_32x32x32_bf16 v[56:71], v[48:51], v[120:127], v28\n"
      "s_mov_b64 exec, s[70:71]\n"
      "ds_read_b128 v[48:51], v29 offset:18048\n"
      "s_mov_b64 exec, -1\n"
      "v_exp_f32 v130, v130\n"
      "v_exp_f32 v131, v131\n"
      "v_exp_f32 v132, v132\n"
      "v_exp_f32 v133, v133\n"
      "s_waitcnt lgkmcnt(3)\n"
      "v_smfmac_f32_32x32x32_bf16 v[72:87], v[52:55], v[112:119], v28\n"
      "v_exp_f32 v134, v134\n"
      "v_exp_f32 v135, v135\n"
      "s_setprio 0\n"
      "v_exp_f32 v136, v136\n"
      "v_exp_f32 v137, v137\n"
      "s_waitcnt lgkmcnt(1)\n"
      "v_smfmac_f32_32x32x32_bf16 v[56:71], v[52:55], v[128:135], v28\n"
      "s_mov_b64 exec, s[70:71]\n"
      "ds_read_b128 v[52:55], v29 offset:19968\n"
      "s_mov_b64 exec, -1\n"
      "v_exp_f32 v138, v138\n"
      "v_exp_f32 v139, v139\n"
      "v_exp_f32 v140, v140\n"
      "v_exp_f32 v141, v141\n"
      "v_mfma_f32_32x32x16_bf16 v[104:119], v[44:47], v[36:39], v[88:103]\n"
      "v_exp_f32 v142, v142\n"
      "v_exp_f32 v143, v143\n"
      "v_exp_f32 v152, v152\n"
      "v_exp_f32 v153, v153\n"
      "v_mfma_f32_32x32x16_bf16 v[120:135], v[44:47], v[32:35], v[88:103]\n"
      "ds_read_b128 v[44:47], v31 offset:9216\n"
      "v_exp_f32 v154, v154\n"
      "v_exp_f32 v155, v155\n"
      "v_exp_f32 v156, v156\n"
      "v_exp_f32 v157, v157\n"
      "s_waitcnt lgkmcnt(2)\n"
      "v_smfmac_f32_32x32x32_bf16 v[72:87], v[48:51], v[136:143], v28\n"
      "ds_read_b32 v25, %[ad]\n"
      "ds_read_b32 v27, v24\n"
      "v_exp_f32 v158, v158\n"
      "v_exp_f32 v159, v159\n"
      "v_exp_f32 v144, v144\n"
      "v_exp_f32 v145, v145\n"
      "v_mfma_f32_32x32x4_2b_bf16 v[104:135], v[42:43], v[40:41], v[104:135]\n"
      "ds_read_b64 v[42:43], v30 offset:11264\n"
      "v_exp_f32 v146, v146\n"
      "v_exp_f32 v147, v147\n"
      "v_exp_f32 v148, v148\n"
      "v_exp_f32 v149, v149\n"
      "v_exp_f32 v150, v150\n"
      "v_exp_f32 v151, v151\n"
      "v_exp_f32 v160, v160\n"
      "v_exp_f32 v161, v161\n"
      "v_smfmac_f32_32x32x32_bf16 v[56:71], v[48:51], v[152:159], v28\n"
      "s_mov_b64 exec, s[70:71]\n"
      "ds_read_b128 v[48:51], v29 offset:20608\n"
      "s_mov_b64 exec, -1\n"
      "v_exp_f32 v162, v162\n"
      "v_exp_f32 v163, v163\n"
      "v_exp_f32 v164, v164\n"
      "v_exp_f32 v165, v165\n"
      "s_waitcnt lgkmcnt(5)\n"
      "v_smfmac_f32_32x32x32_bf16 v[72:87], v[52:55], v[144:151], v28\n"
      "s_sext_i32_i8 s64, s60\n"
      "s_mul_i32 s64, s64, 23040\n"
      "s_lshr_b64 s[60:61], s[60:61], 8\n"
      "v_add_u32 v31, s64, v31\n"
      "v_add_u32 v30, s64, v30\n"
      "v_add_u32 v29, s64, v29\n"
      "s_add_u32 s62, s62, 1\n"
      "s_cmp_lt_u32 s62, 9\n"
      "v_exp_f32 v166, v166\n"
      "v_exp_f32 v167, v167\n"
      "s_cbranch_scc1 Lloop%=\n"
      "s_setprio 3\n"
      "v_xor_b32 v24, 16, %[sw]\n"
      "v_smfmac_f32_32x32x32_bf16 v[56:71], v[52:55], v[160:167], v28\n"
      "v_subrev_u32 v31, %[gb], %[lk]\n"
      "s_waitcnt lgkmcnt(0)\n"
      "v_cmp_gt_u32 vcc, 8, v25\n"
      "v_add_u32 v29, %[xw], v31\n"
      "v_mov_b32 v26, 9\n"
      "v_lshrrev_b32 v30, 1, v31\n"
      "s_mov_b32 s61, 0x5d800000\n"
      "v_add_u32 v28, %[xr], v31\n"
      "s_cbranch_vccnz Lsb%=\n"
      "Lbb%=:\n"
      "ds_read_b32 v27, v24\n"
      "ds_read_b128 v[104:107], v28 offset:0\n"
      "ds_read_b128 v[108:111], v28 offset:1024\n"
      "ds_read_b128 v[112:115], v28 offset:2048\n"
      "ds_write_b128 v29, v[56:59] offset:0\n"
      "ds_write_b128 v29, v[60:63] offset:1024\n"
      "ds_write_b128 v29, v[64:67] offset:2048\n"
      "ds_write_b32 %[sw], v26\n"
      "s_waitcnt lgkmcnt(4)\n"
      "v_cmp_gt_u32 vcc, 9, v27\n"
      "s_cbranch_vccnz Lsc%=\n"
      "Lbc%=:\n"
      "s_setprio 3\n"
      "v_add_f32 v80, v80, v112\n"
      "v_add_f32 v72, v72, v104\n"
      "v_mov_b32 v27, v80\n"
      "v_mov_b32 v25, v80\n"
      "v_add_f32 v73, v73, v105\n"
      "v_add_f32 v74, v74, v106\n"
      "v_permlane32_swap_b32 v27, v25\n"
      "v_add_f32 v75, v75, v107\n"
      "v_add_f32 v76, v76, v108\n"
      "v_rcp_f32 v27, v25\n"
      "v_add_f32 v77, v77, v109\n"
      "v_add_f32 v78, v78, v110\n"
      "v_add_f32 v79, v79, v111\n"
      "v_add_f32 v81, v81, v113\n"
      "v_add_f32 v82, v82, v114\n"
      "v_add_f32 v83, v83, v115\n"
      "v_add_f32 v25, v25, v27\n"
      "v_mul_f32 v72, v72, v27\n"
      "v_mul_f32 v73, v73, v27\n"
      "v_mul_f32 v74, v74, v27\n"
      "v_mul_f32 v75, v75, v27\n"
      "v_mul_f32 v76, v76, v27\n"
      "v_mul_f32 v77, v77, v27\n"
      "v_mul_f32 v78, v78, v27\n"
      "v_mul_f32 v79, v79, v27\n"
      "v_mul_f32 v80, v80, v27\n"
      "v_mul_f32 v81, v81, v27\n"
      "v_mul_f32 v82, v82, v27\n"
      "v_mul_f32 v83, v83, v27\n"
      "v_fma_f32 v25, abs(v72), abs(v73), v25\n"
      "v_cvt_pk_bf16_f32 v44, v72, v73\n"
      "v_fma_f32 v25, abs(v74), abs(v75), v25\n"
      "v_cvt_pk_bf16_f32 v45, v74, v75\n"
      "v_fma_f32 v25, abs(v76), abs(v77), v25\n"
      "v_cvt_pk_bf16_f32 v46, v76, v77\n"
      "v_fma_f32 v25, abs(v78), abs(v79), v25\n"
      "v_cvt_pk_bf16_f32 v47, v78, v79\n"
      "v_fma_f32 v25, abs(v80), abs(v81), v25\n"
      "v_cvt_pk_bf16_f32 v42, v80, v81\n"
      "v_fma_f32 v25, abs(v82), abs(v83), v25\n"
      "v_cvt_pk_bf16_f32 v43, v82, v83\n"
      "v_cmp_nge_f32 vcc, s61, v25\n"
      "v_min_u32 v44, -2, v44\n"
      "v_min_u32 v45, -2, v45\n"
      "v_min_u32 v46, -2, v46\n"
      "v_min_u32 v47, -2, v47\n"
      "v_min_u32 v42, -2, v42\n"
      "v_min_u32 v43, -2, v43\n"
      "s_mov_b64 %[bad], vcc\n"
      "s_cbranch_vccnz Lend%=\n"
      "buffer_store_dwordx4 v[44:47], v31, %[rs], %[om] offen sc1\n"
      "s_mov_b32 exec_hi, 0\n"
      "buffer_store_dwordx2 v[42:43], v30, %[rs], %[orr] offen sc1\n"
      "s_mov_b32 exec_hi, -1\n"
      "s_branch Lend%=\n"
      "Lsa%=:\n"
      "s_sleep 1\n"
      "ds_read_b32 v27, v24\n"
      "s_waitcnt lgkmcnt(0)\n"
      "v_cmp_ne_u32 vcc, %[tg], v27\n"
      "s_sub_u32 s66, s66, 1\n"
      "s_cbranch_scc1 Lsa%=_to\n"
      "s_cbranch_vccnz Lsa%=\n"
      "s_branch Lba%=\n"
      "Lsa%=_to:\n"
      "s_mov_b32 s66, 0\n"
      "s_branch Lba%=\n"
      "Lsb%=:\n"
      "s_sleep 1\n"
      "ds_read_b32 v27, %[ad]\n"
      "s_waitcnt lgkmcnt(0)\n"
      "v_cmp_gt_u32 vcc, 8, v27\n"
      "s_sub_u32 s66, s66, 1\n"
      "s_cbranch_scc1 Lsb%=_to\n"
      "s_cbranch_vccnz Lsb%=\n"
      "s_branch Lbb%=\n"
      "Lsb%=_to:\n"
      "s_mov_b32 s66, 0\n"
      "s_branch Lbb%=\n"
      "Lsc%=:\n"
      "s_setprio 0\n"
      "ds_read_b32 v27, v24\n"
      "ds_read_b128 v[104:107], v28 offset:0\n"
      "ds_read_b128 v[108:111], v28 offset:1024\n"
      "ds_read_b128 v[112:115], v28 offset:2048\n"
      "s_waitcnt lgkmcnt(0)\n"
      "v_cmp_gt_u32 vcc, 9, v27\n"
      "s_cbranch_vccz Lbc%=\n"
      "s_sub_u32 s66, s66, 1\n"
      "s_cbranch_scc1 Lbc%=\n"
      "s_sleep 1\n"
      "s_branch Lsc%=\n"
      "Lend%=:\n"
      "s_setprio 0\n"
        : [bad] "=&s"(bad)
        : [lq] "v"(lq), [lqr] "v"(lqr), [lk] "v"(lk), [lr] "v"(lr), [lv] "v"(lv), [sr] "v"(sr), [sw] "v"(sw), [lqb] "v"(lqb), [ad] "v"(ad), [one] "v"(one),
          [rs] "s"(srd), [om] "s"(om), [orr] "s"(orr), [gk] "s"(grp * 1024), [gb] "s"(lbase + grp * 1024), [tg] "s"((u32)tag), [mt] "s"(wave ^ 4),
          [xw] "s"(lbase + XCH(wave)), [xr] "s"(lbase + XCH(wave ^ 4))
        : "v24", "v25", "v26", "v27", "v28", "v29", "v30", "v31", "v32", "v33", "v34", "v35", "v36", "v37", "v38", "v39", "v40", "v41", "v42", "v43", "v44", "v45", "v46", "v47", "v48", "v49", "v50", "v51", "v52", "v53", "v54", "v55", "v56", "v57", "v58", "v59", "v60", "v61", "v62", "v63", "v64", "v65", "v66", "v67", "v68", "v69", "v70", "v71", "v72", "v73", "v74", "v75", "v76", "v77", "v78", "v79", "v80", "v81", "v82", "v83", "v84", "v85", "v86", "v87", "v88", "v89", "v90", "v91", "v92", "v93", "v94", "v95", "v96", "v97", "v98", "v99", "v100", "v101", "v102", "v103", "v104", "v105", "v106", "v107", "v108", "v109", "v110", "v111", "v112", "v113", "v114", "v115", "v116", "v117", "v118", "v119", "v120", "v121", "v122", "v123", "v124", "v125", "v126", "v127", "v128", "v129", "v130", "v131", "v132", "v133", "v134", "v135", "v136", "v137", "v138", "v139", "v140", "v141", "v142", "v143", "v144", "v145", "v146", "v147", "v148", "v149", "v150", "v151", "v152", "v153", "v154", "v155", "v156", "v157", "v158", "v159", "v160", "v161", "v162", "v163", "v164", "v165", "v166", "v167", "s60", "s61", "s62", "s63", "s64", "s65", "s66", "s67", "s68", "s69", "s70", "s71", "vcc", "scc", "memory");
      LANEVARS
      STAMP(6);
      if (__builtin_expect(bad != 0, 0)) {
        const u32 qr = plR * 512 + tok * 16 + 8 * odd;
        const f32x16 o = attn_exact(rs, WS_QP + tq * 40960, plA * 512 + lane * 16, qr, qr + hh * 40960, hh * 10240 + vcol(tok) * 16, tok < 20);
        const float inv = __builtin_amdgcn_rcpf(halves(o[8]).y);
        u32 pv[6];
#pragma unroll
        for (int j = 0; j < 6; ++j) pv[j] = tmin(pk(o[2 * j] * inv, o[2 * j + 1] * inv), ~1u);
        st128((u32x4){pv[0], pv[1], pv[2], pv[3]}, srd, lane * 16, om);
        __builtin_amdgcn_raw_buffer_store_b64((u32x2){pv[4], pv[5]}, rs, hh ? WS_END : tok * 8, orr, SC1);
      }
    }
  }

}

extern "C" void kernel_launch(void* const* d_in, const int* in_sizes, int n_in, void* d_out, int out_size,
                              void* d_ws, size_t ws_size, hipStream_t stream) {
  fused_block<<<256, 768, 0, stream>>>((const char*)d_in[0], (const char*)d_in[1], (const char*)d_in[2], (const char*)d_in[4],
                                       (const char*)d_in[5], (const char*)d_in[6], (const char*)d_in[7], (char*)d_out, (char*)d_ws);
}
```
